```python
import torch

def _softmax(x, axis=-1):
    return torch.softmax(x, dim=axis)
def _gelu(x):
    return torch.nn.functional.gelu(x, approximate="tanh")
def _silu(x):
    return torch.nn.functional.silu(x)
def _sigmoid(x):
    return torch.sigmoid(x)
def _layernorm(x, axis=-1, eps=1e-5):
    m = x.mean(dim=axis, keepdim=True)
    v = x.var(dim=axis, keepdim=True, unbiased=False)
    return (x - m) / torch.sqrt(v + eps)
def _scatter_add(tgt, idx, src, axis=0):
    return tgt.clone().index_add_(axis, idx, src)
def _segment_sum(src, seg, n):
    out = torch.zeros((n,) + src.shape[1:], dtype=src.dtype)
    return out.index_add_(0, seg, src)
def _pad(x, pw):
    flat = tuple(v for pair in reversed(pw) for v in pair)
    return torch.nn.functional.pad(x, flat)

kernel_name = 'parallel_sum__mha_block__swiglu_ffn__prompt'

def setup_inputs():

    _w = (lambda a: (lambda t: t.to(torch.bfloat16) if t.is_floating_point() else t)(torch.as_tensor(a)))
    def _A_setup():
        B, S, D, H = 1, 2048, 512, 32
        return {
            "x": _w((lambda *s: torch.randn(*s, dtype=torch.bfloat16) * 0.05)(B, S, D)),
            "wqkv": _w((lambda *s: torch.randn(*s, dtype=torch.bfloat16) * 0.2)(D, 3 * D)),
            "wo": _w((lambda *s: torch.randn(*s, dtype=torch.bfloat16) * 0.1)(D, D)),
        }
    _A_in = _A_setup()

    def _B_setup():
        B, S, D, F = 1, 2048, 512, 1792
        return {
            "x": _w((lambda *s: torch.randn(*s, dtype=torch.bfloat16) * 0.05)(B, S, D)),
            "w_gate": _w((lambda *s: torch.randn(*s, dtype=torch.bfloat16) * 0.1)(D, F)),
            "w_up": _w((lambda *s: torch.randn(*s, dtype=torch.bfloat16) * 0.1)(D, F)),
            "w_down": _w((lambda *s: torch.randn(*s, dtype=torch.bfloat16) * 0.1)(F, D)),
        }
    _B_in = _B_setup()

    return {
        "A_x": _A_in["x"],
        "A_wqkv": _A_in["wqkv"],
        "A_wo": _A_in["wo"],
        "B_x": _B_in["x"],
        "B_w_gate": _B_in["w_gate"],
        "B_w_up": _B_in["w_up"],
        "B_w_down": _B_in["w_down"],
    }

def reference(A_x, A_wqkv, A_wo, B_x, B_w_gate, B_w_up, B_w_down):
    def _A_ref(x, wqkv, wo):
        B, S, D = x.shape
        H = 32
        Dh = D // H
        qkv = (lambda x, *d: x.permute(*d))((x @ wqkv).reshape(B, S, 3, H, Dh), 2, 0, 3, 1, 4)
        q, k, v = qkv[0], qkv[1], qkv[2]
        scores = torch.einsum("bhqd,bhkd->bhqk", q, k) * (Dh ** -0.5)
        p = _softmax(scores, axis=-1)
        o = torch.einsum("bhqk,bhkd->bhqd", p, v)
        return (lambda x, *d: x.permute(*d))(o, 0, 2, 1, 3).reshape(B, S, D) @ wo

    def _B_ref(x, w_gate, w_up, w_down):
        g = _silu(x @ w_gate)
        u = x @ w_up
        return (g * u) @ w_down

    x = A_x
    xn = _layernorm(x, axis=-1)
    return x + _A_ref(xn, A_wqkv, A_wo) + _B_ref(xn, B_w_gate, B_w_up, B_w_down)
```

```cpp
#include <hip/hip_runtime.h>
#include <hip/hip_bf16.h>
typedef unsigned short u16;
__device__ __forceinline__ float bf2f(u16 v){ return __uint_as_float(((unsigned)v)<<16); }
__device__ __forceinline__ u16 f2bf(float f){ unsigned u=__float_as_uint(f); u += 0x7fffu + ((u>>16)&1u); return (u16)(u>>16); }
constexpr int S=2048, D=512, H=32, DH=16, F=1792;

__global__ void ln_k(const u16* x, u16* xn){
  int row=blockIdx.x, t=threadIdx.x;
  float v[8]; float s=0;
  for(int i=0;i<8;++i){ v[i]=bf2f(x[row*D+t*8+i]); s+=v[i]; }
  for(int o=32;o;o>>=1) s+=__shfl_xor(s,o);
  float m=s/D; float q=0;
  for(int i=0;i<8;++i){ float d=v[i]-m; q+=d*d; }
  for(int o=32;o;o>>=1) q+=__shfl_xor(q,o);
  float r=rsqrtf(q/D+1e-5f);
  for(int i=0;i<8;++i) xn[row*D+t*8+i]=f2bf((v[i]-m)*r);
}
__global__ void gemm_k(const u16* A, const u16* B, float* C, int M,int N,int K){
  __shared__ float As[16][17], Bs[16][17];
  int tx=threadIdx.x, ty=threadIdx.y; int r=blockIdx.y*16+ty, c=blockIdx.x*16+tx; float acc=0;
  for(int k0=0;k0<K;k0+=16){ As[ty][tx]=bf2f(A[r*K+k0+tx]); Bs[ty][tx]=bf2f(B[(k0+ty)*N+c]); __syncthreads();
    for(int k=0;k<16;++k) acc+=As[ty][k]*Bs[k][tx]; __syncthreads(); }
  C[r*N+c]=acc;
}
__global__ void attn_k(const float* qkv, u16* o){
  int h=blockIdx.y; int row=blockIdx.x*64+threadIdx.x;
  float q[16]; for(int d=0;d<16;++d) q[d]=bf2f(f2bf(qkv[row*1536+h*16+d]*0.25f*1.4426950408889634f));
  float m=-1e30f,l=0,acc[16]; for(int d=0;d<16;++d) acc[d]=0;
  for(int j=0;j<S;++j){ float s=0; for(int d=0;d<16;++d) s+=q[d]*bf2f(f2bf(qkv[j*1536+512+h*16+d]));
    float mn=fmaxf(m,s); float a=exp2f(m-mn); float p=bf2f(f2bf(exp2f(s-mn))); l=l*a+p;
    for(int d=0;d<16;++d) acc[d]=acc[d]*a+p*bf2f(f2bf(qkv[j*1536+1024+h*16+d])); m=mn; }
  for(int d=0;d<16;++d) o[row*D+h*16+d]=f2bf(acc[d]/l);
}
__global__ void h_k(const float* g,const float* u,u16* h,int n){ int i=blockIdx.x*256+threadIdx.x; if(i<n){ float a=g[i]; h[i]=f2bf(a/(1.f+__expf(-a))*u[i]); } }
__global__ void fin_k(const u16* x,const float* a,const float* b,u16* out,int n){ int i=blockIdx.x*256+threadIdx.x; if(i<n) out[i]=f2bf(bf2f(x[i])+a[i]+b[i]); }

extern "C" void kernel_launch(void* const* d_in, const int* in_sizes, int n_in, void* d_out, int out_size, void* d_ws, size_t ws_size, hipStream_t stream){
  const u16* x=(const u16*)d_in[0]; const u16* wqkv=(const u16*)d_in[1]; const u16* wo=(const u16*)d_in[2];
  const u16* wg=(const u16*)d_in[4]; const u16* wu=(const u16*)d_in[5]; const u16* wd=(const u16*)d_in[6];
  char* ws=(char*)d_ws;
  u16* xn=(u16*)ws; ws+=S*D*2; float* qkv=(float*)ws; ws+=S*1536*4; u16* o=(u16*)ws; ws+=S*D*2;
  float* g=(float*)ws; ws+=S*F*4; float* u=(float*)ws; ws+=S*F*4; u16* h=(u16*)ws; ws+=S*F*2; float* a=(float*)ws; ws+=S*D*4; float* b=(float*)ws;
  ln_k<<<S,64,0,stream>>>(x,xn);
  gemm_k<<<dim3(1536/16,S/16),dim3(16,16),0,stream>>>(xn,wqkv,qkv,S,1536,D);
  attn_k<<<dim3(S/64,H),64,0,stream>>>(qkv,o);
  gemm_k<<<dim3(D/16,S/16),dim3(16,16),0,stream>>>(o,wo,a,S,D,D);
  gemm_k<<<dim3(F/16,S/16),dim3(16,16),0,stream>>>(xn,wg,g,S,F,D);
  gemm_k<<<dim3(F/16,S/16),dim3(16,16),0,stream>>>(xn,wu,u,S,F,D);
  h_k<<<S*F/256,256,0,stream>>>(g,u,h,S*F);
  gemm_k<<<dim3(D/16,S/16),dim3(16,16),0,stream>>>(h,wd,b,S,D,F);
  fin_k<<<S*D/256,256,0,stream>>>(x,a,b,(u16*)d_out,S*D);
}
```

```cpp
#include <hip/hip_runtime.h>
#include <utility>

typedef float f32x16 __attribute__((ext_vector_type(16)));
typedef float f32x4 __attribute__((ext_vector_type(4)));
typedef float f32x2 __attribute__((ext_vector_type(2)));
typedef __bf16 bf16x2 __attribute__((ext_vector_type(2)));
typedef __bf16 bf16x8 __attribute__((ext_vector_type(8)));
typedef unsigned u32x4 __attribute__((ext_vector_type(4)));
typedef unsigned u32x2 __attribute__((ext_vector_type(2)));
typedef int i32x4 __attribute__((ext_vector_type(4)));
typedef short s16x4 __attribute__((ext_vector_type(4)));
typedef unsigned long long u64;
#define LAS __attribute__((address_space(3)))
#define GAS __attribute__((address_space(1)))
typedef LAS char* ldsp;
#define DI __device__ __forceinline__
#define SBAR() __builtin_amdgcn_sched_barrier(0)
#define PIN(x) asm volatile("" : "+v"(x))
#define MFMA(a, b, c) __builtin_amdgcn_mfma_f32_32x32x16_bf16(__builtin_bit_cast(bf16x8, a), __builtin_bit_cast(bf16x8, b), c, 0, 0, 0)
#define EX2(x) __builtin_amdgcn_exp2f(x)
#define RLX_AGENT __ATOMIC_RELAXED, __HIP_MEMORY_SCOPE_AGENT
#define RFL(x) __builtin_amdgcn_readfirstlane(x)

#define GEMM_PRIO 1
constexpr int SEQ = 2048, DM = 512, FF = 1792;
constexpr size_t WS_EPOCH = 0;
constexpr size_t WS_FLAG = 4096;
constexpr size_t WS_KV = 1 << 20;
constexpr size_t WS_H = WS_KV + (4 << 20);
constexpr size_t WS_O = WS_H + (8 << 20);
constexpr size_t WS_STAMP = WS_O + (4 << 20);
constexpr int FLAG_KV = 0, FLAG_H = 256, FLAG_O = 512;
constexpr int SLOT = 16896, L_W = 0, L_SCR = 8 * SLOT  , SCRW = 4224, L_XCH = L_SCR  , L_MISC = L_SCR + 18432  ;
constexpr int L_RSTD = L_MISC  , L_PROG = L_MISC + 512  , L_CNT = L_PROG + 16  , L_XF = L_CNT + 16  , LDS_BYTES = L_MISC + 1024;
constexpr int G3RING = 33792, G3PIECE = 2176, G3RC = 15;
constexpr float LOG2E = 1.4426950408889634f;
constexpr unsigned SPIN_CAP = 1u << 20;

#define STAMP(k) do {} while (0)
#define STAMPC(k) do {} while (0)
#define STAMP_DECL
#define STAMP_FLUSH do {} while (0)

template <int N, class Fn, int... I> DI void sfor_(Fn&& f, std::integer_sequence<int, I...>) { (f(std::integral_constant<int, I>{}), ...); }
template <int N, class Fn> DI void sfor(Fn&& f) { sfor_<N>(f, std::make_integer_sequence<int, N>{}); }

DI unsigned cvtpk(float lo, float hi) { f32x2 v = {lo, hi}; bf16x2 r = __builtin_convertvector(v, bf16x2); return __builtin_bit_cast(unsigned, r); }
DI float dot2(unsigned a, unsigned b, float c) { return __builtin_amdgcn_fdot2_f32_bf16(__builtin_bit_cast(bf16x2, a), __builtin_bit_cast(bf16x2, b), c, false); }
DI u32x4 centre8(u32x4 x, float nm) {
  u32x4 c; float t0, t1, t2, t3, t4, t5, t6, t7;
  asm("v_dot2_f32_bf16 %4, %12, %16, %18\n\tv_dot2_f32_bf16 %5, %12, %17, %18\n\tv_dot2_f32_bf16 %6, %13, %16, %18\n\tv_dot2_f32_bf16 %7, %13, %17, %18\n\t"
      "v_dot2_f32_bf16 %8, %14, %16, %18\n\tv_dot2_f32_bf16 %9, %14, %17, %18\n\tv_dot2_f32_bf16 %10, %15, %16, %18\n\tv_dot2_f32_bf16 %11, %15, %17, %18\n\t"
      "v_cvt_pk_bf16_f32 %0, %4, %5\n\tv_cvt_pk_bf16_f32 %1, %6, %7\n\tv_cvt_pk_bf16_f32 %2, %8, %9\n\tv_cvt_pk_bf16_f32 %3, %10, %11"
      : "=&v"(c[0]), "=&v"(c[1]), "=&v"(c[2]), "=&v"(c[3]), "=&v"(t0), "=&v"(t1), "=&v"(t2), "=&v"(t3), "=&v"(t4), "=&v"(t5), "=&v"(t6), "=&v"(t7)
      : "v"(x[0]), "v"(x[1]), "v"(x[2]), "v"(x[3]), "s"(0x00003f80u), "s"(0x3f800000u), "v"(nm));
  return c;
}
DI i32x4 make_srd(const void* p, unsigned bytes) {
  u64 a = (u64)p; i32x4 r; r[0] = RFL((unsigned)a); r[1] = RFL((unsigned)(a >> 32)) & 0xffff; r[2] = bytes; r[3] = 0x20000; return r;
}
DI __amdgpu_buffer_rsrc_t make_rsrc(const void* p, unsigned bytes) { return __builtin_amdgcn_make_buffer_rsrc((void*)p, 0, bytes, 0x20000); }
DI void dma16(unsigned m0v, unsigned voff, i32x4 srd, unsigned soff) {
  asm volatile("s_mov_b32 m0, %0\n\ts_nop 0\n\tbuffer_load_dwordx4 %1, %2, %3 offen lds" :: "s"(m0v), "v"(voff), "s"(srd), "s"(soff) : "memory");
}
template <int N> DI void wait_vm() { asm volatile("s_waitcnt vmcnt(%0)" :: "i"(N) : "memory"); }
template <int N> DI void wait_vm2(u32x4& a, u32x4& b) { asm volatile("s_waitcnt vmcnt(%2)" : "+v"(a), "+v"(b) : "i"(N) : "memory"); }
DI void ld2_sc1(u32x4& a, u32x4& b, unsigned voff, i32x4 srd, unsigned s0, unsigned s1) {
  asm volatile("buffer_load_dwordx4 %0, %2, %3, %4 offen sc1\n\tbuffer_load_dwordx4 %1, %2, %3, %5 offen sc1"
               : "=&v"(a), "=&v"(b) : "v"(voff), "s"(srd), "s"(s0), "s"(s1) : "memory");
}
DI void st_sc1(u32x4 v, i32x4 srd, unsigned voff, unsigned soff) {
  asm volatile("buffer_store_dwordx4 %0, %1, %2, %3 offen sc1\n\ts_nop 1" :: "v"(v), "v"(voff), "s"(srd), "s"(soff) : "memory");
}
DI void wait_lgkm0() { asm volatile("s_waitcnt lgkmcnt(0)" ::: "memory"); }
DI void wg_barrier() { asm volatile("s_waitcnt lgkmcnt(0)\n\ts_barrier" ::: "memory"); }
DI u32x2 trread(ldsp p) { return __builtin_bit_cast(u32x2, __builtin_amdgcn_ds_read_tr16_b64_v4i16((LAS s16x4*)p)); }
DI u32x4 wfrag(ldsp p0, ldsp p1) { u32x2 a = trread(p0), b = trread(p1); return (u32x4){a[0], a[1], b[0], b[1]}; }
DI u32x4 pack8(const f32x16& a, int o, float s) {
  return (u32x4){cvtpk(a[o] * s, a[o + 1] * s), cvtpk(a[o + 2] * s, a[o + 3] * s), cvtpk(a[o + 4] * s, a[o + 5] * s), cvtpk(a[o + 6] * s, a[o + 7] * s)};
}
DI f32x16 zero16() { f32x16 z; for (int i = 0; i < 16; ++i) z[i] = 0.f; return z; }
DI void wait_flags(char* ws, int first, int stride, int n, u64 token, int lane) {
  GAS u64* f = (GAS u64*)(ws + WS_FLAG + (size_t)(first + (lane < n ? lane : 0) * stride) * 128);
  _Pragma("nounroll") for (unsigned spins = 0; spins < SPIN_CAP; ++spins) {
    u64 v = __hip_atomic_load(f, RLX_AGENT);
    if (__all(v == token)) break;
    __builtin_amdgcn_s_sleep(2);
  }
  asm volatile("" ::: "memory");
}
DI void set_flag(char* ws, int idx, u64 token) { __hip_atomic_store((GAS u64*)(ws + WS_FLAG + (size_t)idx * 128), token, RLX_AGENT); }
DI ldsp opaque(ldsp p) { asm volatile("" : "+v"(p)); return p; }
DI void PINV(u32x4& v) { asm volatile("" : "+v"(v)); }
DI unsigned lds_ld(ldsp p) { return *(volatile LAS unsigned*)p; }
DI void lds_st(ldsp p, unsigned v) { *(volatile LAS unsigned*)p = v; }

struct Args { const char* x; const char* wqkv; const char* wo; const char* wg; const char* wu; const char* wd; char* out; char* ws; };

template <int n> DI void dma_stage(unsigned ldsW, int wq, int rot, int cs, i32x4 srdG, i32x4 srdU, unsigned voffA, unsigned voffB0, unsigned voffB1, unsigned voffB2) {
  const unsigned base = ldsW + ((6 + n) & 7) * SLOT;
  const unsigned krow = (unsigned)(((n + rot) & 7) * 64);
  if constexpr (n < 8) {
    const unsigned so = (krow + 16 * wq) * (FF * 2) + cs * 128, lb = base + wq * 4224;
    dma16(lb, voffA, srdG, so); dma16(lb + 1056, voffA, srdG, so + 2 * FF * 2);
    dma16(lb + 2112, voffA, srdU, so); dma16(lb + 3168, voffA, srdU, so + 2 * FF * 2);
  } else {
    const unsigned so = (krow + 32 * (wq >> 1)) * (FF * 2) + 2048 + cs * 96, lb = base + (wq >> 1) * 6272 + (wq & 1) * 3200;
    i32x4 srd = (wq & 1) ? srdU : srdG;
    dma16(lb, voffB0, srd, so); dma16(lb + 1024, voffB1, srd, so); dma16(lb + 2048, voffB2, srd, so);
  }
}
constexpr int g2_pieces(int s) { return s < 8 ? 4 : 3; }
constexpr int g2_wait(int n) { int mx = n + 6 > 7 ? n + 6 : 7; if (mx > 15) mx = 15; int c = 0; for (int s = n + 2; s <= mx; ++s) c += g2_pieces(s); return c; }
constexpr int g3_ops(int i) { return (i + 8 < 28 ? 2 : 0) + (i >= 1 && i + 14 < 36 ? 2 : 0); }
constexpr int g3_wait(int j) {
  int c = 0;
  if (j < 8) { c = 2 * (7 - j); for (int i = 0; i < j; ++i) c += g3_ops(i); }
  else { int i0 = j - 8; c = (i0 >= 1 && i0 + 14 < 36 ? 2 : 0); for (int i = i0 + 1; i < j; ++i) c += g3_ops(i); }
  return c;
}

DI void gemm_waves(const Args& A, ldsp lds, const int w, const int lane, const int wid, const int rb, const int cs, const int tb, const int nb, const int rot) {
  char* ws = A.ws;
  const unsigned lds0 = (unsigned)(uintptr_t)lds;
  const int t32 = lane & 31, hh = lane >> 5, cg = (lane >> 4) & 1, q4 = (lane >> 2) & 3, p4 = lane & 3;
  STAMP_DECL
  STAMP(0);
  const u64 ep = __hip_atomic_load((GAS u64*)(ws + WS_EPOCH), RLX_AGENT);
  u32x4 xr[8][4];
  {
    const auto rx = make_rsrc(A.x, SEQ * DM * 2);
    const unsigned vo = (unsigned)((rb * 128 + 32 * w + (lane >> 3)) * 1024 + (lane & 7) * 16);
    sfor<8>([&](auto m) { sfor<4>([&](auto r) {
      xr[m][r] = __builtin_amdgcn_raw_buffer_load_b128(rx, vo, r * 8192 + ((m + rot) & 7) * 128, 0); }); });
  }
  const u64 token = (((u64)(unsigned)RFL((unsigned)(ep >> 32)) << 32) | (u64)(unsigned)RFL((unsigned)ep)) + 1;
  float nmean[4];
  {
    float s1[4] = {0.f, 0.f, 0.f, 0.f}, s2[4] = {0.f, 0.f, 0.f, 0.f};
    sfor<8>([&](auto m) { sfor<4>([&](auto r) { sfor<4>([&](auto d) {
      s1[r] = dot2(xr[m][r][+d], 0x3f803f80u, s1[r]); s2[r] = dot2(xr[m][r][+d], xr[m][r][+d], s2[r]); }); }); });
    sfor<4>([&](auto r) {
      float a = s1[r], b = s2[r];
      a += __builtin_bit_cast(float, __builtin_amdgcn_update_dpp(0, __builtin_bit_cast(int, a), 0xB1, 0xf, 0xf, true));
      b += __builtin_bit_cast(float, __builtin_amdgcn_update_dpp(0, __builtin_bit_cast(int, b), 0xB1, 0xf, 0xf, true));
      a += __builtin_bit_cast(float, __builtin_amdgcn_update_dpp(0, __builtin_bit_cast(int, a), 0x4E, 0xf, 0xf, true));
      b += __builtin_bit_cast(float, __builtin_amdgcn_update_dpp(0, __builtin_bit_cast(int, b), 0x4E, 0xf, 0xf, true));
      a += __builtin_bit_cast(float, __builtin_amdgcn_update_dpp(0, __builtin_bit_cast(int, a), 0x141, 0xf, 0xf, true));
      b += __builtin_bit_cast(float, __builtin_amdgcn_update_dpp(0, __builtin_bit_cast(int, b), 0x141, 0xf, 0xf, true));
      const float mean = a * (1.f / DM), var = fmaxf(b * (1.f / DM) - mean * mean, 0.f);
      nmean[r] = -mean;
      if ((lane & 7) == 0) *(LAS float*)(lds + L_RSTD + w * 128 + (8 * r + (lane >> 3)) * 4) = __builtin_amdgcn_rsqf(var + 1e-5f);
    });
  }
  STAMP(1);
  u32x4 xf[32];
  ldsp scr = lds + L_SCR + w * SCRW;
  ldsp sw = scr + (lane & 7) * 528 + (lane >> 3) * 16, sr = scr + hh * 528 + t32 * 16;
  sfor<2>([&](auto m) {
    sfor<4>([&](auto r) { *(LAS u32x4*)(sw + r * 128) = centre8(xr[m][r], nmean[r]); });
    sfor<4>([&](auto s) { xf[4 * m + s] = *(LAS u32x4*)(sr + s * 1056); });
  });
  wg_barrier();
  STAMP(2);
  const float rstd = *(LAS float*)(lds + L_RSTD + w * 128 + t32 * 4);
  ldsp wq0 = lds + L_W + (8 * hh + q4) * 192 + cg * 32 + p4 * 8;
  ldsp wq1 = opaque(wq0 + 16 * 3072);
  f32x16 accK = zero16(), accV = zero16();
  {
    u32x4 wk[2], wv[2];
    auto rdw = [&](auto ks) { ldsp b = (ks < 16 ? wq0 : wq1) + (ks & 15) * 3072; wk[ks & 1] = wfrag(b, b + 768); wv[ks & 1] = wfrag(b + 64, b + 768 + 64); };
    rdw(std::integral_constant<int, 0>{});
    sfor<32>([&](auto ks) {
      constexpr int m = ks >> 2, s = ks & 3;
      if constexpr (ks < 31) rdw(std::integral_constant<int, ks + 1>{});
      SBAR();
      accK = MFMA(wk[ks & 1], xf[ks], accK);
      accV = MFMA(xf[ks], wv[ks & 1], accV);
      SBAR();
      if constexpr (m < 6) {
        *(LAS u32x4*)(sw + s * 128) = centre8(xr[m + 2][s], nmean[s]);
        if constexpr (s == 3) sfor<4>([&](auto i) { xf[4 * (m + 2) + i] = *(LAS u32x4*)(sr + i * 1056); });
      }
      SBAR();
    });
  }
  STAMP(3);
  {
    const i32x4 rkv = make_srd(ws + WS_KV, 4 << 20);
    const unsigned tile = (unsigned)(((2 * cs) * 64 + 4 * rb + w) * 2048);
    st_sc1(pack8(accK, 0, rstd), rkv, lane * 16, tile);
    st_sc1(pack8(accK, 8, rstd), rkv, lane * 16, tile + 64 * 2048);
    sfor<4>([&](auto g) { const f32x4 rr = *(LAS f32x4*)(lds + L_RSTD + w * 128 + (8 * g + 4 * hh) * 4);
      sfor<4>([&](auto i) { accV[4 * g + i] *= rr[+i]; }); });
    const unsigned vo = (unsigned)(cg * (64 * 2048) + 1024 + hh * 256 + (lane & 15) * 16);
    st_sc1(pack8(accV, 0, 1.f), rkv, vo, tile);
    st_sc1(pack8(accV, 8, 1.f), rkv, vo + 512, tile);
  }
  STAMP(4);
  {
    f32x16 accQ = zero16();
    sfor<32>([&](auto ks) { ldsp b = (ks < 16 ? wq0 : wq1) + (ks & 15) * 3072 + 128; accQ = MFMA(wfrag(b, b + 768), xf[ks], accQ); });
    const float qs = rstd * (0.25f * LOG2E);
    *(LAS u32x4*)(scr + lane * 16) = pack8(accQ, 0, qs);
    *(LAS u32x4*)(scr + 1024 + lane * 16) = pack8(accQ, 8, qs);
  }
  STAMP(5);
  wait_vm<0>();
  wg_barrier();
  if (w == 0 && lane == 0) set_flag(ws, FLAG_KV + cs * 16 + rb, token);
  STAMP(6);
  const i32x4 srdG = make_srd(A.wg, DM * FF * 2), srdU = make_srd(A.wu, DM * FF * 2);
  const unsigned voffA = (unsigned)((4 * (lane >> 4) + ((lane >> 3) & 1)) * (FF * 2) + (lane & 7) * 16);
  const unsigned voffB0 = (unsigned)((lane / 6) * (FF * 2) + (lane % 6) * 16), voffB1 = (unsigned)(((lane + 64) / 6) * (FF * 2) + ((lane + 64) % 6) * 16),
                 voffB2 = (unsigned)(((lane + 128) / 6) * (FF * 2) + ((lane + 128) % 6) * 16);
  sfor<6>([&](auto i) { dma_stage<i + 2>(lds0 + L_W, w, rot, cs, srdG, srdU, voffA, voffB0, voffB1, voffB2); });
  const i32x4 rh = make_srd(ws + WS_H, 64 * 112 * 1024);
  const unsigned hrow = (unsigned)((4 * rb + w) * 112 * 1024);
  ldsp rdA = lds + L_W + (cg * 2 + (q4 >> 1)) * 1056 + (4 * hh + (q4 & 1)) * 128 + p4 * 8;
  ldsp rdB = lds + L_W + cg * 3200 + (8 * hh + q4) * 96 + p4 * 8;
  f32x16 acc[4];
  u32x4 wf[2][4];
  ldsp rdS = rdA;
  auto rdfrags = [&](auto kk) {
    constexpr int n = kk >> 2, s = kk & 3;
    if constexpr (s == 0) rdS = opaque((n < 8 ? rdA : rdB) + ((6 + n) & 7) * SLOT);
    if constexpr (n < 8) { ldsp b = rdS + s * 4224; sfor<4>([&](auto T) { wf[kk & 1][T] = wfrag(b + T * 32, b + T * 32 + 256); }); }
    else { ldsp b = rdS + (s >> 1) * 6272 + (s & 1) * 1536; sfor<3>([&](auto T) { wf[kk & 1][T] = wfrag(b + T * 32, b + T * 32 + 384); }); }
  };
  auto h_epilogue = [&](auto NT, unsigned ks0) {
    const float c1 = -rstd * LOG2E, c2 = rstd * rstd;
    sfor<NT>([&](auto T) {
      float hv[8];
      sfor<8>([&](auto i) { const float g = acc[T][+i], u = acc[T][i + 8]; hv[i] = (g * u) * (c2 * __builtin_amdgcn_rcpf(1.f + EX2(g * c1))); });
      const u32x4 pk = {cvtpk(hv[0], hv[1]), cvtpk(hv[2], hv[3]), cvtpk(hv[4], hv[5]), cvtpk(hv[6], hv[7])};
      st_sc1(pk, rh, lane * 16, hrow + (ks0 + T) * 1024);
    });
  };
  rdfrags(std::integral_constant<int, 0>{});
  sfor<16>([&](auto n) {
    constexpr int NT = n < 8 ? 4 : 3;
    if constexpr (n == 0 || n == 8) sfor<4>([&](auto T) { acc[T] = zero16(); });
    sfor<4>([&](auto s) {
      constexpr int kk = 4 * n + s;
      if constexpr (s == 0 && n >= 1 && n < 15) { wait_vm<g2_wait(n)>(); lds_st(lds + L_PROG + w * 4, n); }
      if constexpr (s == 3 && n >= 1 && n < 15) {
        _Pragma("nounroll") for (unsigned spins = 0; spins < SPIN_CAP; ++spins) {
          const u32x4 pr = *(volatile LAS u32x4*)(lds + L_PROG);
          const unsigned mn = RFL(min(min(pr[0], pr[1]), min(pr[2], pr[3])));
          if (mn >= (unsigned)n) break;
          __builtin_amdgcn_s_sleep(1);
        }
        asm volatile("" ::: "memory");
        if constexpr (n + 7 < 16) dma_stage<n + 7>(lds0 + L_W, w, rot, cs, srdG, srdU, voffA, voffB0, voffB1, voffB2);
      }
      if constexpr (kk + 1 < 64) rdfrags(std::integral_constant<int, kk + 1>{});
      SBAR();
      sfor<NT>([&](auto T) { acc[T] = MFMA(wf[kk & 1][T], xf[kk & 31], acc[T]); });
      SBAR();
    });
    if constexpr (n == 7) { h_epilogue(std::integral_constant<int, 4>{}, 4 * cs); STAMP(7); }
  });
  STAMP(8);
  lds_st(lds + L_PROG + w * 4, 16);
  _Pragma("nounroll") for (unsigned spins = 0; spins < SPIN_CAP; ++spins) {
    const u32x4 pr = *(volatile LAS u32x4*)(lds + L_PROG);
    const unsigned mn = RFL(min(min(pr[0], pr[1]), min(pr[2], pr[3])));
    if (mn >= 16u) break;
    __builtin_amdgcn_s_sleep(1);
  }
  asm volatile("" ::: "memory");
  const i32x4 srdD = make_srd(A.wd, FF * DM * 2), srdO = make_srd(A.wo, DM * DM * 2);
  const unsigned voffD = (unsigned)((4 * (lane >> 4) + ((lane >> 3) & 1)) * 1024 + (lane & 7) * 16);
  const unsigned ring = lds0 + L_W + w * G3RING;
  auto ksof = [&](int j) -> unsigned { return j < 16 ? 16 * w + j : (j < 28 ? 64 + 12 * w + (j - 16) : 8 * w + (j - 28)); };
  auto dma_w3 = [&](auto j) {
    const unsigned so = ksof(j) * 16 * 1024 + nb * 128, lb = ring + (j % G3RC) * G3PIECE;
    if constexpr (j < 28) { dma16(lb, voffD, srdD, so); dma16(lb + 1088, voffD, srdD, so + 2048); }
    else { dma16(lb, voffD, srdO, so); dma16(lb + 1088, voffD, srdO, so + 2048); }
  };
  sfor<G3RC>([&](auto j) { dma_w3(j); });
  h_epilogue(std::integral_constant<int, 3>{}, 64 + 3 * cs);
  wait_vm<0>();
  {
    unsigned old = 0;
    if (lane == 0) old = __hip_atomic_fetch_add((LAS unsigned*)(lds + L_CNT), 1u, __ATOMIC_RELAXED, __HIP_MEMORY_SCOPE_WORKGROUP);
    if (lane == 0 && old == 3) set_flag(ws, FLAG_H + rb * 16 + cs, token);
  }
  STAMP(9);
  wait_flags(ws, FLAG_H + (tb >> 1) * 16 + 4 * w, 1, 4, token, lane);
  STAMP(10);
  const i32x4 srdH = make_srd(ws + WS_H, 64 * 112 * 1024), srdOf = make_srd(ws + WS_O, 64 * 32 * 1024);
  const unsigned l16 = lane * 16;
  u32x4 bf[8][2];
  auto ld_frag = [&](auto j) {
    if constexpr (j < 28) { const unsigned so = (2 * tb * 112 + ksof(j)) * 1024; ld2_sc1(bf[j & 7][0], bf[j & 7][1], l16, srdH, so, so + 112 * 1024); }
    else { const unsigned so = (2 * tb * 32 + ksof(j)) * 1024; ld2_sc1(bf[j & 7][0], bf[j & 7][1], l16, srdOf, so, so + 32 * 1024); }
  };
  sfor<8>([&](auto j) { ld_frag(j); });
  sfor<4>([&](auto T) { acc[T] = zero16(); });
  ldsp rd3 = opaque(lds + L_W + w * G3RING + (q4 >> 1) * 1088 + (2 * hh + (q4 & 1)) * 128 + cg * 32 + p4 * 8);
  auto step3 = [&](auto j, auto NW) {
    wait_vm2<NW>(bf[j & 7][0], bf[j & 7][1]);
    ldsp b = rd3 + (j % G3RC) * G3PIECE;
    const u32x4 w0 = wfrag(b, b + 512), w1 = wfrag(b + 64, b + 64 + 512);
    acc[0] = MFMA(w0, bf[j & 7][0], acc[0]); acc[1] = MFMA(w1, bf[j & 7][0], acc[1]);
    acc[2] = MFMA(w0, bf[j & 7][1], acc[2]); acc[3] = MFMA(w1, bf[j & 7][1], acc[3]);
    SBAR();
  };
  sfor<28>([&](auto j) {
    step3(j, std::integral_constant<int, g3_wait(j)>{});
    if constexpr (j + 8 < 28) ld_frag(std::integral_constant<int, j + 8>{});
    if constexpr (j >= 1 && j + 14 < 36) dma_w3(std::integral_constant<int, j + 14>{});
    SBAR();
  });
  STAMP(11);
  const int tid = w * 64 + lane;
  const char* xrow = A.x + (size_t)(64 * tb + (tid >> 3)) * 1024 + nb * 128 + (tid & 7) * 16;
  const u32x4 xa = *(const u32x4*)xrow, xb = *(const u32x4*)(xrow + 32 * 1024);
  wait_flags(ws, FLAG_O + (tb >> 1) * 16 + 4 * w, 1, 4, token, lane);
  wait_vm<0>();
  STAMP(12);
  sfor<8>([&](auto i) { ld_frag(std::integral_constant<int, 28 + i>{}); });
  sfor<8>([&](auto i) { step3(std::integral_constant<int, 28 + i>{}, std::integral_constant<int, 14 - 2 * i>{}); });
  STAMP(13);
  {
    ldsp pw = lds + L_W + w * G3RING + t32 * 272 + hh * 16;
    sfor<4>([&](auto a) { sfor<4>([&](auto g) {
      *(LAS f32x4*)(pw + (a >> 1) * (32 * 272) + (a & 1) * 128 + g * 32) = (f32x4){acc[a][4 * g], acc[a][4 * g + 1], acc[a][4 * g + 2], acc[a][4 * g + 3]}; }); });
    wg_barrier();
    char* orow = A.out + (size_t)(64 * tb + (tid >> 3)) * 1024 + nb * 128 + (tid & 7) * 16;
    sfor<2>([&](auto it) {
      ldsp pr = lds + L_W + ((tid >> 3) + 32 * it) * 272 + (tid & 7) * 32;
      f32x4 s0 = *(LAS f32x4*)pr, s1 = *(LAS f32x4*)(pr + 16);
      sfor<3>([&](auto k) { s0 += *(LAS f32x4*)(pr + (k + 1) * G3RING); s1 += *(LAS f32x4*)(pr + (k + 1) * G3RING + 16); });
      const u32x4 xv = it ? xb : xa;
      u32x4 o;
      o[0] = cvtpk(s0[0] + __uint_as_float(xv[0] << 16), s0[1] + __uint_as_float(xv[0] & 0xffff0000u));
      o[1] = cvtpk(s0[2] + __uint_as_float(xv[1] << 16), s0[3] + __uint_as_float(xv[1] & 0xffff0000u));
      o[2] = cvtpk(s1[0] + __uint_as_float(xv[2] << 16), s1[1] + __uint_as_float(xv[2] & 0xffff0000u));
      o[3] = cvtpk(s1[2] + __uint_as_float(xv[3] << 16), s1[3] + __uint_as_float(xv[3] & 0xffff0000u));
      *(u32x4*)(orow + it * 32 * 1024) = o;
    });
  }
  if (blockIdx.x == 0 && w == 0 && lane == 0) __hip_atomic_store((GAS u64*)(ws + WS_EPOCH), token, RLX_AGENT);
  STAMP(14);
  STAMP_FLUSH;
}

DI void kv_load(u32x4& k, u32x4& v0, u32x4& v1, i32x4 srd, unsigned koff, unsigned voff, unsigned soff) {
  asm volatile("buffer_load_dwordx4 %0, %3, %5, %6 offen sc1\n\t"
               "s_mov_b64 exec, %7\n\t"
               "buffer_load_dwordx4 %1, %4, %5, %6 offen offset:1024 sc1\n\t"
               "buffer_load_dwordx4 %2, %4, %5, %6 offen offset:1536 sc1\n\t"
               "s_mov_b64 exec, -1"
               : "=&v"(k), "+v"(v0), "+v"(v1) : "v"(koff), "v"(voff), "s"(srd), "s"(soff), "s"(0x0000ffff0000ffffull) : "memory");
}
template <int N> DI void kv_wait(u32x4& k, u32x4& v0, u32x4& v1) { asm volatile("s_waitcnt vmcnt(%3)" : "+v"(k), "+v"(v0), "+v"(v1) : "i"(N) : "memory"); }
DI float tilemax(const f32x16& s, float m) {
#pragma unroll
  for (int r = 0; r < 16; r += 2) m = fmaxf(m, fmaxf(s[r], s[r + 1]));
  return m;
}
DI float halfmax(float m) {
  auto rr = __builtin_amdgcn_permlane32_swap(__float_as_uint(m), __float_as_uint(m), false, false);
  return fmaxf(__uint_as_float(rr[0]), __uint_as_float(rr[1]));
}

#define E_(SC, i) do { float t_ = EX2(SC[i]); PIN(t_); SC[i] = t_; SBAR(); } while (0)
#define C_(dst, j, SC, i) do { unsigned t_ = cvtpk(SC[i], SC[i + 1]); PIN(t_); dst[j] = t_; SBAR(); } while (0)
#define UNIT(SC, SN, KN, QN, CN, O, V0, OP, VP) do { \
  E_(SC, 0); C_(ph, 2, SN, 12); E_(SC, 1); C_(ph, 3, SN, 14); SN = MFMA(KN, QN, CN); SBAR(); \
  E_(SC, 2); E_(SC, 3); C_(pl, 0, SC, 0); E_(SC, 4); E_(SC, 5); \
  OP = MFMA(VP, ph, OP); SBAR(); \
  C_(pl, 1, SC, 2); E_(SC, 6); E_(SC, 7); C_(pl, 2, SC, 4); E_(SC, 8); E_(SC, 9); C_(pl, 3, SC, 6); E_(SC, 10); E_(SC, 11); \
  O = MFMA(V0, pl, O); SBAR(); \
  C_(ph, 0, SC, 8); E_(SC, 12); E_(SC, 13); C_(ph, 1, SC, 10); E_(SC, 14); E_(SC, 15); \
} while (0)

DI void attn_waves(const Args& A, ldsp lds, const int a, const int lane, const int wid, const int rb, const int cs, const int rot) {
  char* ws = A.ws;
  const unsigned lds0 = (unsigned)(uintptr_t)lds;
  STAMP_DECL
  STAMP(0);
  const u64 ep = __hip_atomic_load((GAS u64*)(ws + WS_EPOCH), RLX_AGENT);
  const u64 token = (((u64)(unsigned)RFL((unsigned)(ep >> 32)) << 32) | (u64)(unsigned)RFL((unsigned)ep)) + 1;
  {
    const i32x4 srdQ = make_srd(A.wqkv, DM * 3 * DM * 2);
    unsigned vq[3];
    sfor<3>([&](auto j) { const int c = 64 * j + lane, row = c / 12, jj = c % 12, seg = jj >> 2;
      vq[j] = (unsigned)(row * 3072 + (seg == 2 ? 0 : (seg + 1) * 1024) + cs * 64 + (jj & 3) * 16); });
    sfor<8>([&](auto m) { const unsigned so = (unsigned)((((m + rot) & 7) * 64 + 16 * a) * 3072), lb = lds0 + L_W + (64 * m + 16 * a) * 192;
      sfor<3>([&](auto j) { dma16(lb + j * 1024, vq[j], srdQ, so); }); });
    const i32x4 srdG = make_srd(A.wg, DM * FF * 2), srdU = make_srd(A.wu, DM * FF * 2);
    const unsigned voffA = (unsigned)((4 * (lane >> 4) + ((lane >> 3) & 1)) * (FF * 2) + (lane & 7) * 16);
    dma_stage<0>(lds0 + L_W, a, rot, cs, srdG, srdU, voffA, 0, 0, 0);
    dma_stage<1>(lds0 + L_W, a, rot, cs, srdG, srdU, voffA, 0, 0, 0);
  }
  if (a == 0 && lane < 12) lds_st(lds + L_PROG + lane * 4, 0u);
  STAMP(1);
  wait_vm<8>();
  wg_barrier();
  STAMP(2);
  wait_vm<0>();
  wg_barrier();
  STAMP(3);
  const int e = a & 1, u = a >> 1, head = 2 * cs + e;
  u32x4 q[4];
  sfor<4>([&](auto i) { q[i] = *(LAS u32x4*)(lds + L_SCR + ((i + 2 * u) & 3) * SCRW + e * 1024 + lane * 16); });
  wait_flags(ws, FLAG_KV + cs * 16, 1, 16, token, lane);
  STAMP(4);
  const i32x4 srd = make_srd(ws + WS_KV + (size_t)head * 64 * 2048, 64 * 2048);
  const unsigned koff = lane * 16, voff = (lane & 15) * 16 + (lane >> 5) * 256;
  const unsigned ones = 0x3f803f80u;
  u32x4 k0, k1, k2, va0 = {ones, ones, ones, ones}, vb0 = va0, va1 = va0, vb1 = va0, va2 = va0, vb2 = va0;
  const unsigned tbase = (unsigned)(4 * rb + u) * 2048;
#define TOFF(i) ((tbase + (unsigned)(i) * 4096u) & (64u * 2048u - 1u))
  f32x16 c[4], o[4];
  float mref[4];
  bool exact = false;
  for (;;) {
    kv_load(k0, va0, vb0, srd, koff, voff, TOFF(0));
    kv_load(k1, va1, vb1, srd, koff, voff, TOFF(1));
    kv_load(k2, va2, vb2, srd, koff, voff, TOFF(2));
    kv_wait<3>(k0, va0, vb0); kv_wait<3>(k1, va1, vb1);
    if (!exact) sfor<4>([&](auto i) { mref[i] = halfmax(tilemax(MFMA(k1, q[i], zero16()), tilemax(MFMA(k0, q[i], zero16()), -INFINITY))) + 80.f; });
    sfor<4>([&](auto i) { const float nm = -mref[i]; for (int r = 0; r < 16; ++r) { c[i][r] = nm; o[i][r] = 0.f; } PIN(c[i]); });
    u32x4 pl, ph = {0, 0, 0, 0};
    f32x16 sa = MFMA(k0, q[0], c[0]), sb = zero16();
    STAMP(5); STAMPC(9);
#define STEP(KC, VAC, VBC, KN, VAN, VBN, KP, VAP, VBP, I, DOLOAD) do { \
      UNIT(sa, sb, KC, q[1], c[1], o[0], VAC, o[3], VBP); \
      if (DOLOAD) kv_load(KP, VAP, VBP, srd, koff, voff, TOFF((I) + 2)); \
      SBAR(); \
      UNIT(sb, sa, KC, q[2], c[2], o[1], VAC, o[0], VBC); \
      UNIT(sa, sb, KC, q[3], c[3], o[2], VAC, o[1], VBC); \
      kv_wait<3>(KN, VAN, VBN); SBAR(); \
      UNIT(sb, sa, KN, q[0], c[0], o[3], VAC, o[2], VBC); \
    } while (0)
    STEP(k0, va0, vb0, k1, va1, vb1, k2, va2, vb1, 0, false);
    for (int i = 1; i < 31; i += 3) {
      STEP(k1, va1, vb1, k2, va2, vb2, k0, va0, vb0, i, true);
      STEP(k2, va2, vb2, k0, va0, vb0, k1, va1, vb1, i + 1, true);
      STEP(k0, va0, vb0, k1, va1, vb1, k2, va2, vb2, i + 2, true);
    }
    STEP(k1, va1, vb1, k2, va2, vb2, k0, va0, vb0, 31, false);
    ph[2] = cvtpk(sb[12], sb[13]); ph[3] = cvtpk(sb[14], sb[15]);
    o[3] = MFMA(vb1, ph, o[3]);
    kv_wait<0>(k2, va2, vb2);
    STAMP(6); STAMPC(10);
    if (exact) break;
    bool bad = false;
    sfor<4>([&](auto i) { float t = o[i][8]; for (int r = 0; r < 8; ++r) t = fmaxf(t, fabsf(o[i][r])); bad |= !(t < 1e36f);
                          for (int r = 0; r < 9; ++r) bad |= (o[i][r] != o[i][r]); });
    if (__builtin_expect(!__any(bad), 1)) break;
    exact = true;
    sfor<4>([&](auto i) { mref[i] = -INFINITY; });
    for (int i = 0; i < 32; ++i) {
      kv_load(k0, va0, vb0, srd, koff, voff, TOFF(i)); kv_wait<0>(k0, va0, vb0);
      sfor<4>([&](auto t) { mref[t] = tilemax(MFMA(k0, q[t], zero16()), mref[t]); });
    }
    sfor<4>([&](auto i) { mref[i] = halfmax(mref[i]); });
  }
  float wgt[4];
  sfor<4>([&](auto i) { const float l = o[i][8], inv = __builtin_amdgcn_rcpf(l); wgt[i] = __builtin_amdgcn_logf(l) + mref[i];
                        for (int r = 0; r < 8; ++r) o[i][r] *= inv; });
  {
    ldsp mine = lds + L_XCH + a * 4608 + lane * 4, other = lds + L_XCH + (a ^ 2) * 4608 + lane * 4;
    sfor<2>([&](auto i) { sfor<8>([&](auto r) { *(LAS float*)(mine + (i * 9 + r) * 256) = o[2 + i][+r]; }); *(LAS float*)(mine + (i * 9 + 8) * 256) = wgt[2 + i]; });
    wait_lgkm0();
    lds_st(lds + L_XF + a * 4, 1u);
    _Pragma("nounroll") for (unsigned spins = 0; spins < SPIN_CAP; ++spins) { if (RFL(lds_ld(lds + L_XF + (a ^ 2) * 4))) break; __builtin_amdgcn_s_sleep(1); }
    asm volatile("" ::: "memory");
    const i32x4 ro = make_srd(ws + WS_O, 64 * 32 * 1024);
    sfor<2>([&](auto i) {
      const float w2 = *(LAS float*)(other + (i * 9 + 8) * 256), wm = fmaxf(wgt[i], w2);
      float a1 = EX2(wgt[i] - wm), a2 = EX2(w2 - wm); const float inv = __builtin_amdgcn_rcpf(a1 + a2); a1 *= inv; a2 *= inv;
      float r8[8]; sfor<8>([&](auto r) { r8[r] = a1 * o[i][+r] + a2 * *(LAS float*)(other + (i * 9 + r) * 256); });
      const u32x4 pk = {cvtpk(r8[0], r8[1]), cvtpk(r8[2], r8[3]), cvtpk(r8[4], r8[5]), cvtpk(r8[6], r8[7])};
      st_sc1(pk, ro, lane * 16, (unsigned)(((4 * rb + 2 * u + i) * 32 + head) * 1024));
    });
  }
  STAMP(7);
  wait_vm<0>();
  {
    unsigned old = 0;
    if (lane == 0) old = __hip_atomic_fetch_add((LAS unsigned*)(lds + L_CNT + 4), 1u, __ATOMIC_RELAXED, __HIP_MEMORY_SCOPE_WORKGROUP);
    if (lane == 0 && old == 3) set_flag(ws, FLAG_O + rb * 16 + cs, token);
  }
  STAMP(8);
  STAMP_FLUSH;
}

__global__ __launch_bounds__(512, 2) void fused_block_kernel(Args A) {
  extern __shared__ __attribute__((aligned(16))) char smem[];
  ldsp lds = (ldsp)smem;
  const int lane = threadIdx.x & 63, wid = RFL(threadIdx.x >> 6);
  const int b = blockIdx.x, g = b & 7, j = b >> 3;
  const int rb = 8 * (g >> 2) + (j & 7), cs = 4 * (g & 3) + (j >> 3), tb = 4 * g + (j & 3), nb = j >> 2;
  const int rot = (rb + 2 * cs) & 7;
  if (wid < 4) attn_waves(A, lds, wid, lane, wid, rb, cs, rot);
  else { __builtin_amdgcn_s_setprio(GEMM_PRIO); gemm_waves(A, lds, wid - 4, lane, wid, rb, cs, tb, nb, rot); }
}

extern "C" void kernel_launch(void* const* d_in, const int* in_sizes, int n_in, void* d_out, int out_size, void* d_ws, size_t ws_size, hipStream_t stream) {
  Args A;
  A.x = (const char*)d_in[0]; A.wqkv = (const char*)d_in[1]; A.wo = (const char*)d_in[2];
  A.wg = (const char*)d_in[4]; A.wu = (const char*)d_in[5]; A.wd = (const char*)d_in[6];
  A.out = (char*)d_out; A.ws = (char*)d_ws;
  (void)hipFuncSetAttribute((const void*)fused_block_kernel, hipFuncAttributeMaxDynamicSharedMemorySize, LDS_BYTES);
  fused_block_kernel<<<256, 512, LDS_BYTES, stream>>>(A);
}
```

```cpp
#include <hip/hip_runtime.h>
#include <utility>

typedef float f32x16 __attribute__((ext_vector_type(16)));
typedef float f32x4 __attribute__((ext_vector_type(4)));
typedef float f32x2 __attribute__((ext_vector_type(2)));
typedef __bf16 bf16x2 __attribute__((ext_vector_type(2)));
typedef __bf16 bf16x8 __attribute__((ext_vector_type(8)));
typedef unsigned u32x4 __attribute__((ext_vector_type(4)));
typedef unsigned u32x2 __attribute__((ext_vector_type(2)));
typedef int i32x4 __attribute__((ext_vector_type(4)));
typedef short s16x4 __attribute__((ext_vector_type(4)));
typedef unsigned long long u64;
#define LAS __attribute__((address_space(3)))
#define GAS __attribute__((address_space(1)))
typedef LAS char* ldsp;
#define DI __device__ __forceinline__
#define SBAR() __builtin_amdgcn_sched_barrier(0)
#define PIN(x) asm volatile("" : "+v"(x))
#define MFMA(a, b, c) __builtin_amdgcn_mfma_f32_32x32x16_bf16(__builtin_bit_cast(bf16x8, a), __builtin_bit_cast(bf16x8, b), c, 0, 0, 0)
#define EX2(x) __builtin_amdgcn_exp2f(x)
#define RLX_AGENT __ATOMIC_RELAXED, __HIP_MEMORY_SCOPE_AGENT
#define RFL(x) __builtin_amdgcn_readfirstlane(x)

constexpr int SEQ = 2048, DM = 512, FF = 1792;
constexpr size_t WS_EPOCH = 0;
constexpr size_t WS_FLAG = 4096;
constexpr size_t WS_CSG = 256 << 10;
constexpr size_t WS_KV = 1 << 20;
constexpr size_t WS_H = WS_KV + (4 << 20);
constexpr size_t WS_O = WS_H + (8 << 20);
constexpr int FLAG_KV = 0, FLAG_H0 = 256, FLAG_H1 = 512, FLAG_O = 768;
constexpr int SLOT = 16896, PIECE = 2176;
constexpr int L_G2 = 0;
constexpr int L_G3 = 4 * SLOT;
constexpr int G3RC = 7;
constexpr int WBLK = 4 * PIECE + 4096, L_WB = 2 * SLOT;
constexpr int L_X = L_WB + 3 * WBLK, XBLK = 4608, XRING = 3 * XBLK;
constexpr int L_SCR = 8 * SLOT  , SCRW = 6144, L_XCH = L_SCR, L_MISC = L_SCR + 4 * SCRW  ;
constexpr int L_CSKV = L_X + 4 * XRING;
constexpr int L_CS = L_MISC  , L_STAT = L_CS + 2048  , L_PROG = L_STAT + 1536  ,
              L_CNT = L_PROG + 16  , L_XF = L_PROG + 48  , L_TOK = L_PROG + 64, LDS_BYTES = L_PROG + 80;
static_assert(L_CSKV + 3072 <= L_SCR && G3RC * PIECE <= SLOT && LDS_BYTES <= 163840, "LDS map");
constexpr float LOG2E = 1.4426950408889634f;
constexpr unsigned SPIN_CAP = 1u << 16;

template <int N, class Fn, int... I> DI void sfor_(Fn&& f, std::integer_sequence<int, I...>) { (f(std::integral_constant<int, I>{}), ...); }
template <int N, class Fn> DI void sfor(Fn&& f) { sfor_<N>(f, std::make_integer_sequence<int, N>{}); }

template <int N> using IC = std::integral_constant<int, N>;
DI unsigned cvtpk0(float lo, float hi) { f32x2 v = {lo, hi}; bf16x2 r = __builtin_convertvector(v, bf16x2); return __builtin_bit_cast(unsigned, r); }
DI unsigned cvtpk(float lo, float hi) { asm("" : "+v"(lo)); asm("" : "+v"(hi)); f32x2 v = {lo, hi}; bf16x2 r = __builtin_convertvector(v, bf16x2); return __builtin_bit_cast(unsigned, r); }
DI float dot2(unsigned a, unsigned b, float c) { return __builtin_amdgcn_fdot2_f32_bf16(__builtin_bit_cast(bf16x2, a), __builtin_bit_cast(bf16x2, b), c, false); }
#define MFMA16(a, b, c) __builtin_amdgcn_mfma_f32_16x16x32_bf16(__builtin_bit_cast(bf16x8, a), __builtin_bit_cast(bf16x8, b), c, 0, 0, 0)
DI u32x4 sumsel(int l) { const unsigned v = ((l & 3) == 0 && ((l >> 2) & 1) == ((l >> 4) & 1)) ? 0x3f803f80u : 0u; return (u32x4){v, v, v, v}; }
DI u32x2 split_a(float v) { const unsigned h = cvtpk(v, 0.f); return (u32x2){h | (cvtpk(v - __uint_as_float(h << 16), 0.f) << 16), h}; }
DI u32x2 split_b(float v) { const unsigned h = cvtpk(v, v); return (u32x2){h, cvtpk(v - __uint_as_float(h << 16), 0.f)}; }
DI int fresh_lane() { int l; asm volatile("v_mbcnt_lo_u32_b32 %0, -1, 0\n\tv_mbcnt_hi_u32_b32 %0, -1, %0" : "=v"(l)); return l; }
DI i32x4 make_srd(const void* p, unsigned bytes) {
  u64 a = (u64)p; i32x4 r; r[0] = RFL((unsigned)a); r[1] = RFL((unsigned)(a >> 32)) & 0xffff; r[2] = bytes; r[3] = 0x20000; return r;
}
template <int LI, int SI> DI void dma16(unsigned lb, unsigned voff, i32x4 srd, unsigned sb) {
  unsigned t;
  asm volatile("s_add_u32 m0, %1, %5\n\ts_add_u32 %0, %4, %6\n\tbuffer_load_dwordx4 %2, %3, %0 offen lds"
               : "=&s"(t) : "s"(lb), "v"(voff), "s"(srd), "s"(sb), "i"(LI), "i"(SI) : "memory", "scc");
}
DI u64 sfresh64(u64 x) { asm volatile("" : "+s"(x)); return x; }
DI unsigned sfresh(unsigned x) { asm volatile("" : "+s"(x)); return x; }
DI void dma4(unsigned m0v, unsigned voff, i32x4 srd, unsigned soff) {
  asm volatile("s_mov_b32 m0, %0\n\ts_nop 0\n\tbuffer_load_dword %1, %2, %3 offen lds" :: "s"(m0v), "v"(voff), "s"(srd), "s"(soff) : "memory");
}
DI void st8_sc1(u32x2 v, i32x4 srd, unsigned voff, unsigned soff) { asm volatile("s_mov_b32 exec_hi, 0\n\tbuffer_store_dwordx2 %0, %1, %2, %3 offen sc1\n\ts_mov_b32 exec_hi, -1\n\ts_nop 0" :: "v"(v), "v"(voff), "s"(srd), "s"(soff) : "memory"); }
#define LD8X4 "buffer_load_dwordx2 %0, %4, %5, %6 offen sc1\n\tbuffer_load_dwordx2 %1, %4, %5, %6 offen offset:1024 sc1\n\t" \
              "buffer_load_dwordx2 %2, %4, %5, %6 offen offset:2048 sc1\n\tbuffer_load_dwordx2 %3, %4, %5, %6 offen offset:3072 sc1"
DI void ld8x4_sc1(u32x2 (&g)[4], unsigned voff, i32x4 srd, unsigned soff) {
  asm volatile(LD8X4 : "=&v"(g[0]), "=&v"(g[1]), "=&v"(g[2]), "=&v"(g[3]) : "v"(voff), "s"(srd), "s"(soff) : "memory");
}
template <int N> DI void gather4(u32x2 (&g)[4], unsigned voff, i32x4 srd, unsigned soff, unsigned tag) {
  asm volatile("s_waitcnt vmcnt(%4)" : "+v"(g[0]), "+v"(g[1]), "+v"(g[2]), "+v"(g[3]) : "i"(N) : "memory");
  _Pragma("nounroll") for (unsigned spins = 0; spins < SPIN_CAP; ++spins) {
    if (__builtin_expect(__all(((g[0][1] ^ tag) | (g[1][1] ^ tag) | (g[2][1] ^ tag) | (g[3][1] ^ tag)) == 0u), 1)) break;
    asm volatile(LD8X4 "\n\ts_waitcnt vmcnt(0)" : "=&v"(g[0]), "=&v"(g[1]), "=&v"(g[2]), "=&v"(g[3]) : "v"(voff), "s"(srd), "s"(soff) : "memory");
  }
}
template <int N> DI void wait_vm() { asm volatile("s_waitcnt vmcnt(%0)" :: "i"(N) : "memory"); }
template <int N> DI void wait_vm2(u32x4& a, u32x4& b) { asm volatile("s_waitcnt vmcnt(%2)" : "+v"(a), "+v"(b) : "i"(N) : "memory"); }
template <int SI> DI void ld1_sc1(u32x4& a, unsigned voff, i32x4 srd, unsigned s0) {
  unsigned t; asm volatile("s_add_u32 %1, %4, %5\n\tbuffer_load_dwordx4 %0, %2, %3, %1 offen sc1" : "=&v"(a), "=&s"(t) : "v"(voff), "s"(srd), "s"(s0), "i"(SI) : "memory", "scc");
}
DI void ldx4(u32x2 (&g)[4], unsigned voff, i32x4 srd) {
  asm volatile("buffer_load_dwordx2 %0, %4, %5, 0 offen\n\tbuffer_load_dwordx2 %1, %4, %5, 0 offen offset:16\n\t"
               "buffer_load_dwordx2 %2, %4, %5, 0 offen offset:32\n\tbuffer_load_dwordx2 %3, %4, %5, 0 offen offset:48"
               : "=&v"(g[0]), "=&v"(g[1]), "=&v"(g[2]), "=&v"(g[3]) : "v"(voff), "s"(srd) : "memory");
}
template <int O> DI void st_out(u32x4 v, i32x4 srd, unsigned voff) { asm volatile("buffer_store_dwordx4 %0, %1, %2, 0 offen offset:%3\n\ts_nop 1" :: "v"(v), "v"(voff), "s"(srd), "i"(O) : "memory"); }
template <int N> DI void wait_vm1(u32x4& a) { asm volatile("s_waitcnt vmcnt(%1)" : "+v"(a) : "i"(N) : "memory"); }
DI void st_sc1(u32x4 v, i32x4 srd, unsigned voff, unsigned soff) {
  asm volatile("buffer_store_dwordx4 %0, %1, %2, %3 offen sc1\n\ts_nop 1" :: "v"(v), "v"(voff), "s"(srd), "s"(soff) : "memory");
}
template <int N> DI void wait_vm64(u64& a) { asm volatile("s_waitcnt vmcnt(%1)" : "+v"(a) : "i"(N) : "memory"); }
DI void ldflag(u64& a, unsigned off, const char* base) { asm volatile("global_load_dwordx2 %0, %1, %2 sc1" : "=&v"(a) : "v"(off), "s"(base) : "memory"); }
DI float halfsum(float m) {
  auto rr = __builtin_amdgcn_permlane32_swap(__float_as_uint(m), __float_as_uint(m), false, false);
  return __uint_as_float(rr[0]) + __uint_as_float(rr[1]);
}
DI void wait_lgkm0() { asm volatile("s_waitcnt lgkmcnt(0)" ::: "memory"); }
DI void wg_barrier() { asm volatile("s_waitcnt lgkmcnt(0)\n\ts_barrier" ::: "memory"); }
DI u32x2 trread(ldsp p) { return __builtin_bit_cast(u32x2, __builtin_amdgcn_ds_read_tr16_b64_v4i16((LAS s16x4*)p)); }
DI u32x4 wfrag(ldsp p0, ldsp p1) { u32x2 a = trread(p0), b = trread(p1); return (u32x4){a[0], a[1], b[0], b[1]}; }
DI u32x4 pack8s(const f32x16& a, int o, float s) { return (u32x4){cvtpk((a[o] * s), (a[o + 1] * s)), cvtpk((a[o + 2] * s), (a[o + 3] * s)), cvtpk((a[o + 4] * s), (a[o + 5] * s)), cvtpk((a[o + 6] * s), (a[o + 7] * s))}; }
DI u32x4 pack8(const f32x16& a, int o) { return (u32x4){cvtpk(a[o], a[o + 1]), cvtpk(a[o + 2], a[o + 3]), cvtpk(a[o + 4], a[o + 5]), cvtpk(a[o + 6], a[o + 7])}; }
DI void PINF(float& v) { asm volatile("" : "+v"(v)); }
#define PRIO(n) __builtin_amdgcn_s_setprio(n)
DI f32x16 zero16() { f32x16 z; for (int i = 0; i < 16; ++i) z[i] = 0.f; return z; }
DI void wait_flags(char* ws, int first, unsigned off, u64 token, u64 v) {
  unsigned cnt = SPIN_CAP;
  asm volatile("1:\n\tv_cmp_ne_u64 vcc, %4, %0\n\ts_cbranch_vccz 2f\n\tglobal_load_dwordx2 %0, %2, %3 sc1\n\ts_sub_u32 %1, %1, 1\n\ts_waitcnt vmcnt(0)\n\ts_cbranch_scc0 1b\n2:"
               : "+&v"(v), "+&s"(cnt) : "v"(off), "s"(ws + WS_FLAG + (size_t)first * 128), "s"(token) : "memory", "scc", "vcc");
}
#define LD16 "s_add_u32 %16, %21, 0\n\t" "buffer_load_dwordx4 %0, %19, %20, %16 offen offset:0 sc1\n\t" "buffer_load_dwordx4 %1, %19, %20, %16 offen offset:1024 sc1\n\t" "buffer_load_dwordx4 %2, %19, %20, %16 offen offset:2048 sc1\n\t" "buffer_load_dwordx4 %3, %19, %20, %16 offen offset:3072 sc1\n\t" \
  "s_add_u32 %16, %21, 4096\n\t" "buffer_load_dwordx4 %4, %19, %20, %16 offen offset:0 sc1\n\t" "buffer_load_dwordx4 %5, %19, %20, %16 offen offset:1024 sc1\n\t" "buffer_load_dwordx4 %6, %19, %20, %16 offen offset:2048 sc1\n\t" "buffer_load_dwordx4 %7, %19, %20, %16 offen offset:3072 sc1\n\t" \
  "s_add_u32 %16, %26, 0\n\t" "buffer_load_dwordx4 %8, %19, %20, %16 offen offset:0 sc1\n\t" "buffer_load_dwordx4 %9, %19, %20, %16 offen offset:1024 sc1\n\t" "buffer_load_dwordx4 %10, %19, %20, %16 offen offset:2048 sc1\n\t" "buffer_load_dwordx4 %11, %19, %20, %16 offen offset:3072 sc1\n\t" \
  "s_add_u32 %16, %26, 4096\n\t" "buffer_load_dwordx4 %12, %19, %20, %16 offen offset:0 sc1\n\t" "buffer_load_dwordx4 %13, %19, %20, %16 offen offset:1024 sc1\n\t" "buffer_load_dwordx4 %14, %19, %20, %16 offen offset:2048 sc1\n\t" "buffer_load_dwordx4 %15, %19, %20, %16 offen offset:3072 sc1\n\t" \
  ""
#define LD16_OPS : "+&v"(b[0]), "+&v"(b[1]), "+&v"(b[2]), "+&v"(b[3]), "+&v"(b[4]), "+&v"(b[5]), "+&v"(b[6]), "+&v"(b[7]), "+&v"(b[8]), "+&v"(b[9]), "+&v"(b[10]), "+&v"(b[11]), "+&v"(b[12]), "+&v"(b[13]), "+&v"(b[14]), "+&v"(b[15]), "=&s"(t), "+&v"(fl), "+&s"(cnt) : "v"(voff), "s"(srd), "s"(s0), "s"(early), "v"(fo), "s"(fb), "s"(token), "s"(s1) : "memory", "scc", "vcc"
DI void ld16_if(u32x4 (&b)[16], unsigned voff, i32x4 srd, unsigned s0, unsigned s1, unsigned early, u64& fl, unsigned fo, const char* fb) {
  unsigned t, cnt = 0; const u64 token = 0;
  asm volatile("s_cmp_eq_u32 %22, 0\n\ts_cbranch_scc1 1f\n\t" LD16 "s_branch 2f\n1:\n\tglobal_load_dwordx2 %17, %23, %24 sc1\n2:" LD16_OPS);
}
DI void ld16_late(u32x4 (&b)[16], unsigned voff, i32x4 srd, unsigned s0, unsigned s1, unsigned early, u64& fl, unsigned fo, const char* fb, u64 token) {
  unsigned t, cnt = SPIN_CAP;
  asm volatile("s_cmp_lg_u32 %22, 0\n\ts_cbranch_scc1 3f\n1:\n\ts_waitcnt vmcnt(0)\n\tv_cmp_ne_u64 vcc, %25, %17\n\ts_cbranch_vccz 2f\n\t"
               "global_load_dwordx2 %17, %23, %24 sc1\n\ts_sub_u32 %18, %18, 1\n\ts_cbranch_scc0 1b\n2:\n\t" LD16 "3:" LD16_OPS);
}
DI ldsp opaque(ldsp p) { asm volatile("" : "+v"(p)); return p; }
DI void PINU(unsigned& v) { asm volatile("" : "+v"(v)); }
DI void PIN16(f32x16& v) { asm volatile("" : "+v"(v)); }
DI void PINV(u32x4& v) { asm volatile("" : "+v"(v)); }
DI unsigned lds_ld(ldsp p) { return *(volatile LAS unsigned*)p; }
DI void lds_st(ldsp p, unsigned v) { *(volatile LAS unsigned*)p = v; }

DI void arrive_and_flag(ldsp cnt, char* ws, int idx, u64 token) {
  unsigned t, t2, so; u64 tv;
  asm volatile("s_mov_b64 exec, 1\n\tds_add_rtn_u32 %0, %4, %4\n\tv_mov_b32 %1, 0\n\tv_mov_b64 %3, %7\n\ts_waitcnt lgkmcnt(0)\n\tv_readfirstlane_b32 %2, %0\n\ts_nop 0\n\t"
               "s_cmp_lg_u32 %2, %5\n\ts_cbranch_scc1 1f\n\tglobal_store_dwordx2 %1, %3, %6 sc1\n1:\n\ts_mov_b64 exec, -1"
               : "=&v"(t), "=&v"(t2), "=&s"(so), "=&v"(tv) : "v"(cnt), "s"(3 * RFL((unsigned)(uintptr_t)cnt)), "s"(ws + WS_FLAG + (size_t)idx * 128), "s"(token) : "memory", "scc");
}
DI void rv_signal(ldsp c) { asm volatile("s_mov_b64 exec, 1\n\tds_add_u32 %0, %0\n\ts_mov_b64 exec, -1" :: "v"(c) : "memory"); }
DI unsigned wait_word(ldsp p, unsigned least, unsigned seen = 0) {
  unsigned v, n = 16 * SPIN_CAP;
  asm volatile("s_nop 0\n1:\n\tv_readfirstlane_b32 %1, %0\n\ts_nop 0\n\ts_cmp_ge_u32 %1, %4\n\ts_cbranch_scc1 2f\n\tds_read_b32 %0, %3\n\ts_sub_u32 %2, %2, 1\n\ts_waitcnt lgkmcnt(0)\n\ts_cbranch_scc0 1b\n2:"
               : "+&v"(seen), "=&s"(v), "+&s"(n) : "v"(p), "s"(least) : "memory", "scc");
  return v;
}
DI void wait_sleep(ldsp p) {
  unsigned v, t, n = SPIN_CAP;
  asm volatile("s_setprio 2\n1:\n\tds_read_b32 %0, %3\n\ts_waitcnt lgkmcnt(0)\n\tv_readfirstlane_b32 %1, %0\n\ts_nop 0\n\ts_cmp_lg_u32 %1, 0\n\ts_cbranch_scc1 2f\n\ts_sleep 2\n\ts_sub_u32 %2, %2, 1\n\ts_cbranch_scc0 1b\n2:\n\ts_setprio 0"
               : "=&v"(t), "=&s"(v), "+&s"(n) : "v"(p) : "memory", "scc");
}
DI void rv_wait(ldsp c, unsigned seen, unsigned want) { wait_word(c, want, seen); }
#define PINS(x) asm volatile("" : "+s"(x))
struct Args { const char* x; const char* wqkv; const char* wo; const char* wg; const char* wu; const char* wd; char* out; char* ws; };

constexpr int CODEWARM = 19;
constexpr int G2R = 6;
constexpr int g2_n(int f) { return f < 128 ? f / 16 : 8 + (f - 128) / 12; }
constexpr int g2_s(int f) { return f < 128 ? (f / 4) % 4 : ((f - 128) / 3) % 4; }
constexpr int g2_t(int f) { return f < 128 ? f % 4 : (f - 128) % 3; }
constexpr int g2_pieces(int n) { return n < 8 ? 4 : n < 16 ? 3 : 0; }
constexpr int g2_warm(int n) { return n == 2 || n == 4; }
constexpr int g2_pre(int n) { return g2_warm(n) ? 1 : n == 12 ? 5 : n == 13 ? 4 : 0; }
constexpr int g2_post(int n) { return n >= 1 && n <= G3RC ? 2 : 0; }
constexpr int g2_wait(int n) { return g2_post(n - 2) + g2_pre(n - 1) + g2_pieces(n + 2) + g2_post(n - 1); }
constexpr int DEFER = 12;
constexpr int G3R = 14, G3P = 16, G3E = 8;
constexpr int DTOP = DEFER + G3R - 2, FTOP = DEFER + G3P - 1 < 27 ? DEFER + G3P - 1 : 27;
constexpr int g3_dn(int j) { int hi = 6 + 2 * (j + 1), cap = j - 1 + G3R; return hi < cap ? hi : cap; }
constexpr int g3_eops(int j) { return (G3E + j < G3P ? 2 : 0) + 2 * (g3_dn(j) - (j ? g3_dn(j - 1) : 6)); }
constexpr int g3_lf(int j) { return j + G3P < 28 ? 2 : 0; }
constexpr int g3_ld(int j) { return j + G3R - 1 < 36 ? 2 : 0; }
constexpr int g3_rd(int j) { return j < 24 ? j + 2 : j < 26 ? j + 4 : 2 * j - 21; }
constexpr int g3_lwait(int j) {
  int best = 63;
  if (j > FTOP) { int c = 0; for (int i = j - G3P + 1; i < j; ++i) c += g3_lf(i) + g3_ld(i); best = c < best ? c : best; }
  if (g3_rd(j) > DTOP) { int i0 = g3_rd(j) - G3R + 1, c = g3_lf(i0); for (int i = i0 + 1; i < j; ++i) c += g3_lf(i) + g3_ld(i); best = c < best ? c : best; }
  return best;
}
constexpr int g3_ewait(int j) {
  if (j < 4) return 63; int c = 0; for (int i = j - 3; i < j; ++i) c += g3_eops(i); return c;
}
static_assert(DEFER >= 4 && DEFER <= 12, "defer point");
DI unsigned stage_row(int n, int rot) { return (unsigned)(((n + sfresh(rot)) & 7) * 64 * (FF * 2)); }
template <int SL, int I> DI void dma_p0(unsigned lb, unsigned sb, i32x4 srdG, i32x4 srdU, unsigned voff) {
  dma16<L_G2 + SL * SLOT + I * 1056, 2 * (I & 1) * FF * 2>(lb, voff, I < 2 ? srdG : srdU, sb);
}

DI unsigned off_x(int rb, int a, int l) { return (unsigned)((rb * 128 + 32 * a + (l >> 3)) * 1024 + ((l & 7) ^ (l >> 3)) * 16); }
DI unsigned off_kv(int cs, int a, int l) { return (unsigned)((16 * a + 4 * (l >> 4) + ((l >> 3) & 1)) * 3072 + 1024 + ((l >> 2) & 1) * 1024 + cs * 64 + (l & 3) * 16); }
DI unsigned off_q(int cs, int a, int l) { return (unsigned)((16 * a + (l >> 2)) * 3072 + cs * 64 + (l & 3) * 16); }

DI void gemm_waves(const Args& A, ldsp lds, const int w, const int lane, const int rb, const int cs, const int tb, const int nb, const int rot, const u64 pc) {
  char* ws = A.ws;
  const unsigned lds0 = (unsigned)(uintptr_t)lds;
  const int t32 = lane & 31, hh = lane >> 5, cg = (lane >> 4) & 1, q4 = (lane >> 2) & 3, p4 = lane & 3;
  {
    const unsigned chunk = ((blockIdx.x >> 3) * 4 + w) % CODEWARM;
    dma4(lds0 + L_STAT, (lane & 15) * 128 + chunk * 2048, make_srd((const void*)(pc & ~255ull), CODEWARM * 2048), 0);
  }
  const u64 ep = __hip_atomic_load((GAS u64*)(ws + WS_EPOCH), RLX_AGENT);
  u64 token;
  i32x4 srdG = make_srd(A.wg, DM * FF * 2), srdU = make_srd(A.wu, DM * FF * 2), rkv = make_srd(ws + WS_KV, 4 << 20); PINS(srdG); PINS(srdU); PINS(rkv);
  unsigned tile = (unsigned)(((2 * cs) * 64 + 4 * rb + w) * 2048); PINS(tile);
  const int j4 = cs & 3, w3 = w == 3 ? 0 : w;
  unsigned vo = (unsigned)(cg * (64 * 2048) + 1024 + hh * 256 + (lane & 15) * 16), l16 = lane * 16, lo128 = (lane & 15) * 128, cgo = t32 * 8 + hh * 4096, selv = sumsel(lane)[0];
  unsigned voffA = (unsigned)((4 * (lane >> 4) + ((lane >> 3) & 1)) * (FF * 2) + (lane & 7) * 16), voffD = (unsigned)((4 * (lane >> 4) + ((lane >> 3) & 1)) * 1024 + (lane & 7) * 16), voffB[3];
  sfor<3>([&](auto k) { const int c = lane + 64 * k, r = c / 6, ch = c - 6 * r; voffB[k] = r * (FF * 2) + (j4 * 96 + ch * 16); PINU(voffB[k]); });
  unsigned xo = (unsigned)((64 * tb + 32 * (w >> 1) + t32) * 1024 + nb * 128 + (w & 1) * 64 + hh * 8);
  PIN(vo); PIN(l16); PIN(lo128); PIN(cgo); PIN(selv); PIN(voffA); PIN(voffD); PIN(xo);
  ldsp rdA = opaque(lds + L_G2 + (cg * 2 + (q4 >> 1)) * 1056 + (4 * hh + (q4 & 1)) * 128 + p4 * 8), rdB = opaque(lds + L_G2 + cg * 3200 + (8 * hh + q4) * 96 + p4 * 8);
  ldsp rd3 = opaque(lds + L_G3 + w * SLOT + (q4 >> 1) * 1088 + (2 * hh + (q4 & 1)) * 128 + cg * 32 + p4 * 8 + (w & 1) * 64), pcg = opaque(lds + L_CS + t32 * 16), rvc = opaque(lds + L_PROG);
  ldsp prr = opaque(lds + L_STAT + w * 384 + hh * 16), pcs = opaque(lds + L_CSKV + lane * 16), pst = opaque(lds + L_STAT + w * 384 + t32 * 4);
  ldsp sk = opaque(lds + L_SCR + w * SCRW + lane * 16), sv = opaque(lds + L_SCR + w * SCRW + cg * 2048 + 1024 + hh * 256 + (lane & 15) * 16);
  u32x4 xf[32];
  f32x16 accK, accV;
  float sq[4] = {0.f, 0.f, 0.f, 0.f}, ex2;
  float rstd, c1, ic2; u32x4 nmf;
  {
    ldsp xa[4];
    sfor<4>([&](auto i) { xa[i] = opaque(lds + L_X + w * XRING + (t32 >> 3) * 1152 + (t32 & 7) * 128 + ((2 * i + hh) ^ (t32 & 7)) * 16); });
    ldsp wb = opaque(lds + L_WB + (q4 >> 1) * 1088 + (4 * hh + (q4 & 1)) * 128 + cg * 32 + p4 * 8);
    u32x4 wk[5], wv[5];
    auto rd = [&](auto ks) { xf[ks] = *(LAS u32x4*)(xa[ks & 3] + ((ks >> 2) % 3) * XBLK);
      ldsp b = wb + ((ks >> 2) % 3) * WBLK + (ks & 3) * PIECE; wk[ks % 5] = wfrag(b, b + 256); wv[ks % 5] = wfrag(b + 64, b + 320); SBAR(); };
    auto mk = [&](auto ks) { if constexpr (ks == 0) accK = MFMA(wk[0], xf[0], zero16()); else accK = MFMA(wk[ks % 5], xf[ks], accK); SBAR(); };
    auto mv = [&](auto ks) { if constexpr (ks == 0) accV = MFMA(xf[0], wv[0], zero16()); else accV = MFMA(xf[ks], wv[ks % 5], accV); SBAR(); };
    auto stats = [&](auto m) {
      sfor<16>([&](auto i) { const unsigned v = xf[4 * m + (i >> 2)][i & 3]; sq[i & 3] = dot2(v, v, sq[i & 3]); });
      sfor<4>([&](auto i) { PINF(sq[i]); }); SBAR(); };
    sfor<8>([&](auto m) {
      wg_barrier();
      rd(IC<4 * m>{}); if constexpr (m > 0) mk(IC<4 * m - 1>{});
      rd(IC<4 * m + 1>{}); if constexpr (m > 0) mv(IC<4 * m - 1>{});
      rd(IC<4 * m + 2>{}); rd(IC<4 * m + 3>{});
      if constexpr (m == 7) stats(m);
      sfor<3>([&](auto s) { mk(IC<4 * m + s>{});
        if constexpr (m == 7 && s == 0) { ex2 = halfsum((sq[0] + sq[1]) + (sq[2] + sq[3])) * (1.f / DM); *(LAS float*)(pst + 256) = ex2; SBAR(); }
        mv(IC<4 * m + s>{}); });
      if constexpr (m < 7) stats(m);
      if constexpr (m == 3) { token = sfresh64((((u64)(unsigned)RFL((unsigned)(ep >> 32)) << 32) | (u64)(unsigned)RFL((unsigned)ep)) + 1);
        *(LAS u64*)(lds + L_TOK) = token; SBAR(); }
    });
    mk(IC<31>{}); mv(IC<31>{});
  }
  {
    wg_barrier();
    const float nmean = *(LAS float*)(pst + 128);
    const u32x4 ck = *(LAS u32x4*)pcs, cv = *(LAS u32x4*)(pcs + 1024);
    SBAR();
    { const u32x2 t = split_b(nmean); nmf = (u32x4){t[0], t[1], t[0], t[1]}; }
    accK = MFMA(ck, nmf, accK);
    accV = MFMA(nmf, cv, accV);
    SBAR();
    rstd = __builtin_amdgcn_rsqf(fmaxf(ex2 - nmean * nmean, 0.f) + 1e-5f);
    *(LAS float*)pst = rstd;
    f32x4 rr[4];
    sfor<4>([&](auto g) { rr[g] = *(LAS f32x4*)(prr + g * 32); });
    SBAR();
    const u32x4 k0 = pack8s(accK, 0, rstd); *(LAS u32x4*)sk = k0; st_sc1(k0, rkv, l16, tile); SBAR();
    const u32x4 k1 = pack8s(accK, 8, rstd); *(LAS u32x4*)(sk + 2048) = k1; st_sc1(k1, rkv, l16, tile + 64 * 2048); SBAR();
    sfor<8>([&](auto i) { accV[+i] = (accV[+i] * rr[i >> 2][i & 3]); });
    const u32x4 v0 = pack8(accV, 0); st_sc1(v0, rkv, vo, tile); SBAR();
    wg_barrier();
    sfor<8>([&](auto i) { accV[i + 8] = (accV[i + 8] * rr[2 + (i >> 2)][i & 3]); });
    const u32x4 v1 = pack8(accV, 8); st_sc1(v1, rkv, vo, tile + 512);
    *(LAS u32x4*)sv = v0; *(LAS u32x4*)(sv + 512) = v1;
    wait_lgkm0(); rv_signal(lds + L_PROG + 8);
    nmf = (u32x4){hh ? 0u : nmf[0], hh ? 0u : nmf[1], 0u, 0u}; PINV(nmf);
    c1 = -rstd * LOG2E; ic2 = __builtin_amdgcn_rcpf(rstd * rstd); PINF(c1); PINF(ic2);
  }
  const i32x4 srdD = make_srd(A.wd, FF * DM * 2), srdO = make_srd(A.wo, DM * DM * 2);
  const i32x4 srdH = make_srd(ws + WS_H, 64 * 112 * 1024);
  f32x16 acc[4];
  u64 flv;
  u32x4 bf[2 * G3P];
  u32x2 xr[4];
  const unsigned hb0 = (unsigned)(((2 * tb + (w >> 1)) * 112 + 16 * w) * 1024), hb1 = (unsigned)(((2 * tb + ((w >> 1) ^ 1)) * 112 + 16 * w) * 1024), hd = (48 - 4 * w) * 1024;
  auto ld_frag = [&](auto j) { ld1_sc1<j * 1024>(bf[2 * (j % G3P)], l16, srdH, j < 16 ? hb0 : hb0 + hd); ld1_sc1<j * 1024>(bf[2 * (j % G3P) + 1], l16, srdH, j < 16 ? hb1 : hb1 + hd); };
  const unsigned rv1 = 4 * (lds0 + L_PROG);
  const unsigned lb3 = lds0 + w * SLOT, wdA = w * (16 * 16 * 1024) + nb * 128, wdB = (64 + 12 * w) * (16 * 1024) + nb * 128;
  auto dma_w3h = [&](auto j, auto e) { constexpr int lo = (j % G3R < G3RC ? L_G3 : L_G2) + (j % G3RC) * PIECE + e * 1088;
    if constexpr (j < 28) dma16<lo, (j < 16 ? j : j - 16) * 16 * 1024 + e * 2048>(lb3, voffD, srdD, j < 16 ? wdA : wdB);
    else dma16<lo, (j - 28) * 16 * 1024 + e * 2048>(lb3, voffD, srdO, w * (8 * 16 * 1024) + nb * 128); };
  auto dma_w3 = [&](auto j) { dma_w3h(j, IC<0>{}); dma_w3h(j, IC<1>{}); };
  auto early_loads = [&](auto j) {
    if constexpr (G3E + j < G3P) ld_frag(IC<G3E + j>{});
    constexpr int d0 = j ? g3_dn(j - 1) : 6; sfor<g3_dn(j) - d0>([&](auto i) { dma_w3(IC<d0 + 1 + i>{}); }); };
  {
    unsigned seen = 0;
    const i32x4 srdM = (w & 1) ? srdU : srdG;
    u32x4 wf[G2R], sel, cf;
    f32x4 csum; u32x2 cg4[4]; ldsp rdC;
    const i32x4 srdC = make_srd(ws + WS_CSG, 512 << 10);
    auto frag = [&](ldsp base, auto n, auto s) {
      if constexpr (n < 8) { ldsp b = base + (n & 3) * SLOT + s * 4224; return wfrag(b, b + 256); }
      else { ldsp b = base + (n & 3) * SLOT + (s >> 1) * 6272 + (s & 1) * 1536; return wfrag(b, b + 384); } };
    auto rdfrag = [&](auto f) { wf[f % G2R] = frag((f < 128 ? rdA : rdB) + g2_t(f) * 32, IC<g2_n(f)>{}, IC<g2_s(f)>{}); };
    auto cs_setup = [&](auto P) { rdC = opaque((P ? rdB : rdA) + (P ? w3 : w) * 32); sel = (u32x4){selv, selv, selv, selv}; PINV(sel); cf = frag(rdC, IC<8 * P>{}, IC<0>{}); };
    unsigned lbp = lds0 + w * 4224, sb0 = w * (16 * FF * 2) + cs * 128, sbn = 0;
    auto piece = [&](auto n, auto i) {
      if constexpr (n == 8 && i == 0) { lbp = lds0 + (w >> 1) * 6272 + (w & 1) * 3200; sb0 = 32 * (w >> 1) * (FF * 2) + 2048 + (cs >> 2) * 384; }
      if constexpr (i == 0) sbn = stage_row(n, rot) + sb0;
      if constexpr (n < 8) dma_p0<n & 3, i>(lbp, sbn, srdG, srdU, voffA);
      else dma16<L_G2 + (n & 3) * SLOT + i * 1024, 0>(lbp, voffB[i], srdM, sbn);
    };
    auto h_epilogue = [&](auto P) {
      constexpr int NT = P ? 3 : 4;
      sfor<NT>([&](auto T) { PIN16(acc[T]); });
      gather4<(P ? 4 : 5)>(cg4, cgo, srdC, (unsigned)(((P * 16 + cs) * 16 + (rb & 8)) * 1024 + (P ? w3 : w) * 256), (unsigned)token);
      float c01 = __uint_as_float(cg4[0][0]) + __uint_as_float(cg4[1][0]), c23 = __uint_as_float(cg4[2][0]) + __uint_as_float(cg4[3][0]); PINF(c01);
      { const u32x2 t = split_a(halfsum(c01 + c23)); ldsp pw = pcg + w * 512; *(LAS u32x2*)pw = t; *(volatile LAS u32x2*)(pw + 8) = t; }
      wait_lgkm0(); rv_signal(rvc); rv_wait(rvc, 0, (P ? 16 : 9) * rv1);
      sfor<NT>([&](auto T) { acc[T] = MFMA(*(LAS u32x4*)(pcg + T * 512), nmf, acc[T]); });
      if constexpr (P == 1) { early_loads(IC<0>{}); early_loads(IC<1>{}); }
      sfor<NT>([&](auto T) {
        float z[8], gu[8];
        sfor<8>([&](auto i) { gu[i] = acc[T][+i] * acc[T][i + 8]; PINF(gu[i]); z[i] = acc[T][+i] * c1; PINF(z[i]); }); SBAR();
        sfor<8>([&](auto i) { z[i] = EX2(z[i]); PINF(z[i]); }); SBAR();
        sfor<8>([&](auto i) { z[i] = __builtin_fmaf(z[i], ic2, ic2); PINF(z[i]); }); SBAR();
        sfor<8>([&](auto i) { z[i] = __builtin_amdgcn_rcpf(z[i]); PINF(z[i]); }); SBAR();
        sfor<8>([&](auto i) { z[i] *= gu[i]; PINF(z[i]); }); SBAR();
        const int ks = P ? 64 + 3 * cs + T : 4 * cs + T;
        st_sc1((u32x4){cvtpk0(z[0], z[1]), cvtpk0(z[2], z[3]), cvtpk0(z[4], z[5]), cvtpk0(z[6], z[7])}, srdH, l16, (unsigned)(((4 * rb + w) * 112 + ks) * 1024));
      });
    };
    cs_setup(IC<0>{});
    sfor<G2R - 1>([&](auto f) { rdfrag(f); });
    PRIO(0);
    sfor<224>([&](auto f) {
      constexpr int n = g2_n(f), s = g2_s(f), T = g2_t(f), kk = 4 * n + s, idx = n < 8 ? f & 15 : (f - 128) % 12, last = n < 8 ? 15 : 11;
      if constexpr ((n & 7) == 0 && T == 2) {
        if constexpr (s == 0) csum = MFMA16(sel, cf, ((f32x4){0.f, 0.f, 0.f, 0.f})); else csum = MFMA16(sel, cf, csum);
        SBAR(); if constexpr (s < 3) cf = frag(rdC, IC<n>{}, IC<s + 1>{}); SBAR();
      }
      if constexpr ((n & 7) == 0 && s == 0) acc[T] = MFMA(wf[f % G2R], xf[0], zero16()); else acc[T] = MFMA(wf[f % G2R], xf[kk & 31], acc[T]);
      SBAR();
      if constexpr (idx == 7 && n >= 1 && n < 15) { PRIO(2); rv_wait(rvc, seen, (n <= 8 ? n : n + 1) * rv1); PRIO(0); }
      if constexpr (idx == 7 && n == 0) { wait_vm<0>(); PRIO(2); arrive_and_flag(lds + L_CNT + 12, ws, FLAG_KV + cs * 16 + rb, token);
        PRIO(0);
        sfor<4>([&](auto i) { piece(IC<2>{}, i); }); }
      if constexpr (idx == 5 && n >= 1 && n < 15) seen = lds_ld(rvc);
      if constexpr (f + G2R - 1 < 224) rdfrag(IC<f + G2R - 1>{});
      if constexpr (idx == 3) {
        if constexpr (g2_warm(n)) {
          const unsigned wv = blockIdx.x * 4 + w;
          dma4(lds0 + L_STAT, lo128, n == 2 ? srdD : srdO, n == 2 ? (wv ? wv * (14 * 128) - 256 : 0) : (wv >> 2) * 2048); }
        if constexpr ((n & 7) == 1) { PRIO(2);
          st8_sc1((u32x2){__float_as_uint(csum[0]), (unsigned)token}, srdC, cgo, (unsigned)((((n >> 3) * 16 + cs) * 16 + rb) * 1024 + (n < 8 ? w : w3) * 256)); PRIO(0); }
        if constexpr ((n & 7) == 7) ld8x4_sc1(cg4, cgo, srdC, (unsigned)((((n >> 3) * 16 + cs) * 16 + (rb & 8)) * 1024 + (n < 8 ? w : w3) * 256));
        if constexpr (n == 12) { ldflag(flv, lo128, ws + WS_FLAG + (size_t)(FLAG_H0 + (tb >> 1) * 16) * 128); ldx4(xr, xo, make_srd(A.x, SEQ * DM * 2)); }
        if constexpr (n == 13) { wait_vm64<7>(flv); PRIO(2); wait_flags(ws, FLAG_H0 + (tb >> 1) * 16, lo128, token, flv); PRIO(0); }
      }
      if constexpr (idx == 7 && n == 8) { wait_vm<0>(); PRIO(2); arrive_and_flag(lds + L_CNT, ws, FLAG_H0 + rb * 16 + cs, token); PRIO(0); }
      if constexpr (idx == 7 && n + 3 < 16) { piece(IC<n + 3>{}, IC<0>{}); piece(IC<n + 3>{}, IC<1>{}); }
      if constexpr (idx == 11 && n + 3 < 16) { piece(IC<n + 3>{}, IC<2>{}); if constexpr (n + 3 < 8) piece(IC<n + 3>{}, IC<3>{}); }
      if constexpr (idx == 15 && g2_post(n)) dma_w3(IC<n - 1>{});
      if constexpr (idx == last && n + 1 < 15) { wait_vm<g2_wait(n + 1)>(); rv_signal(rvc); }
      if constexpr (n >= 13 && (idx & 3) == 3 && 3 * (n - 13) + (idx >> 2) < G3E) ld_frag(IC<3 * (n - 13) + (idx >> 2)>{});
      SBAR();
      if constexpr (f == 127) { PRIO(2); h_epilogue(IC<0>{}); cs_setup(IC<1>{}); PRIO(0); }
    });
    PRIO(2); h_epilogue(IC<1>{});
  }
  {
    u32x4 w3[3][2], wo[8][2];
    ldsp rd[2][2] = {{rd3, opaque(rd3 + ((w & 1) ? -64 : 64))}, {opaque(rd3 + (L_G2 - L_G3)), opaque(rd3 + ((w & 1) ? -64 : 64) + (L_G2 - L_G3))}};
    auto wfr = [&](auto j, auto T) { ldsp b = rd[j % G3R >= G3RC][T] + (j % G3RC) * PIECE; return wfrag(b, b + 512); };
    sfor<4>([&](auto i) { w3[i >> 1][i & 1] = wfr(IC<(i >> 1)>{}, IC<i & 1>{}); });
    asm volatile("" : "+v"(xr[0]), "+v"(xr[1]), "+v"(xr[2]), "+v"(xr[3]) :: "memory");
    sfor<8>([&](auto i) { acc[0][2 * i] = __uint_as_float(xr[i >> 1][i & 1] << 16); acc[0][2 * i + 1] = __uint_as_float(xr[i >> 1][i & 1] & 0xffff0000u); });
    PIN16(acc[0]);
    const char* fob = ws + WS_FLAG + (size_t)(FLAG_O + (tb >> 1) * 16 + 4 * w) * 128;
    unsigned fo = lo128 & 0x180; PIN(fo);
    PRIO(0);
    sfor<28>([&](auto j) {
      u32x4 &b0 = bf[2 * (j % G3P)], &b1 = bf[2 * (j % G3P) + 1];
      if constexpr (j == 2) { wait_vm<0>(); PRIO(2); arrive_and_flag(lds + L_CNT + 4, ws, FLAG_H1 + rb * 16 + cs, token); PRIO(0); }
      if constexpr (j == DEFER) {
        PRIO(2); wait_flags(ws, FLAG_H1 + (tb >> 1) * 16, lo128, token, ~token); ldsp xfw = opaque(lds + L_XF + w * 4); PRIO(0);
        constexpr int f0 = G3E + DEFER < G3P ? G3E + DEFER : G3P;
        sfor<FTOP + 1 - f0>([&](auto i) { ld_frag(IC<f0 + i>{}); __builtin_amdgcn_s_sleep(4); });
        sfor<DTOP - g3_dn(DEFER - 1)>([&](auto i) { dma_w3(IC<g3_dn(DEFER - 1) + 1 + i>{}); __builtin_amdgcn_s_sleep(4); });
        wait_vm<0>();
        wait_sleep(xfw);
      }
      wait_vm2<(j < DEFER ? g3_ewait(j) : g3_lwait(j))>(b0, b1);
      SBAR();
      auto gap = [&](auto g) {
        SBAR();
        if constexpr (j + 2 < 28 && g < 2) w3[(j + 2) % 3][g] = wfr(IC<j + 2>{}, g);
        if constexpr ((j == 24 || j == 25) && g >= 2) wo[j - 24][g - 2] = wfr(IC<j + 4>{}, IC<g - 2>{});
        if constexpr (j >= 26) wo[2 * j - 50 + (g >> 1)][g & 1] = wfr(IC<2 * j - 22 + (g >> 1)>{}, IC<g & 1>{});
        if constexpr (j >= DEFER && g3_ld(j) && g >= 2) dma_w3h(IC<j + G3R - 1>{}, IC<g - 2>{});
        SBAR(); };
      acc[0] = MFMA(w3[j % 3][0], b0, acc[0]); gap(IC<0>{});
      if constexpr (j == 0) acc[1] = MFMA(w3[0][1], b0, zero16()); else acc[1] = MFMA(w3[j % 3][1], b0, acc[1]);
      gap(IC<1>{});
      if constexpr (j == 0) acc[2] = MFMA(w3[0][0], b1, zero16()); else acc[2] = MFMA(w3[j % 3][0], b1, acc[2]);
      gap(IC<2>{});
      if constexpr (j == 0) acc[3] = MFMA(w3[0][1], b1, zero16()); else acc[3] = MFMA(w3[j % 3][1], b1, acc[3]);
      gap(IC<3>{});
      if constexpr (j < DEFER) { if constexpr (j >= 2) early_loads(j); }
      else {
        if constexpr (g3_lf(j)) ld_frag(IC<j + G3P>{});
        if constexpr (j == 23) ldflag(flv, fo, fob);
      }
      SBAR();
    });
    PRIO(2);
    const i32x4 srdOf = make_srd(ws + WS_O, 64 * 32 * 1024), srdOut = make_srd(A.out, SEQ * DM * 2);
    const unsigned ob0 = sfresh((unsigned)(((2 * tb + (w >> 1)) * 32 + 8 * w) * 1024)), ob1 = sfresh((unsigned)(((2 * tb + ((w >> 1) ^ 1)) * 32 + 8 * w) * 1024));
    u32x4 of[16];
    sfor<16>([&](auto i) { of[i] = bf[i + 8]; });
    wait_vm64<0>(flv);
    const unsigned early = RFL(__all(flv == token) ? 1 : 0);
    ld16_if(of, l16, srdOf, ob1, ob0, early, flv, fo, fob);
    sfor<4>([&](auto i) { wo[6 + (i >> 1)][i & 1] = wfr(IC<34 + (i >> 1)>{}, IC<i & 1>{}); });
    sfor<8>([&](auto j) { PINV(wo[j][0]); PINV(wo[j][1]); });
    ldsp pw = opaque(lds + L_G3 + w * SLOT + l16), pr[3];
    sfor<3>([&](auto i) { pr[i] = opaque(lds + L_G3 + (w ^ (i + 1)) * SLOT + l16 + i * 4096); });
    unsigned oo = xo + (xo & 8); PIN(oo);
    ld16_late(of, l16, srdOf, ob1, ob0, early, flv, fo, fob, token);
    auto put = [&](auto i, auto g) { *(LAS f32x4*)(pw + (i - 1) * 4096 + g * 1024) = (f32x4){acc[i][4 * g], acc[i][4 * g + 1], acc[i][4 * g + 2], acc[i][4 * g + 3]}; SBAR(); };
    sfor<8>([&](auto j) { wait_vm1<15 - j>(of[j]); SBAR(); acc[3] = MFMA(wo[j][1], of[j], acc[3]); acc[2] = MFMA(wo[j][0], of[j], acc[2]); SBAR(); });
    sfor<8>([&](auto j) { wait_vm1<7 - j>(of[8 + j]); SBAR(); acc[1] = MFMA(wo[j][1], of[8 + j], acc[1]); SBAR(); if constexpr (j >= 2 && j < 6) put(IC<3>{}, IC<j - 2>{}); if constexpr (j >= 6) put(IC<2>{}, IC<j - 6>{}); });
    sfor<8>([&](auto j) { acc[0] = MFMA(wo[j][0], of[8 + j], acc[0]); SBAR(); if constexpr (j < 2) put(IC<2>{}, IC<j + 2>{}); if constexpr (j >= 2 && j < 6) put(IC<1>{}, IC<j - 2>{}); });
    wait_lgkm0(); rv_signal(rvc); rv_wait(rvc, 0, 17 * rv1);
    f32x4 p[12];
    sfor<12>([&](auto k) { p[k] = *(LAS f32x4*)(pr[k % 3] + (k / 3) * 1024); });
    SBAR();
    u32x2 pk[4];
    sfor<4>([&](auto g) { const f32x4 t = ((f32x4){acc[0][4 * g], acc[0][4 * g + 1], acc[0][4 * g + 2], acc[0][4 * g + 3]} + p[3 * g]) + (p[3 * g + 1] + p[3 * g + 2]);
      pk[g] = (u32x2){cvtpk0(t[0], t[1]), cvtpk0(t[2], t[3])}; });
    sfor<2>([&](auto p) {
      const auto r0 = __builtin_amdgcn_permlane32_swap(pk[2 * p][0], pk[2 * p + 1][0], false, false), r1 = __builtin_amdgcn_permlane32_swap(pk[2 * p][1], pk[2 * p + 1][1], false, false);
      st_out<p * 32>((u32x4){r0[0], r1[0], r0[1], r1[1]}, srdOut, oo);
    });
  }
  if (blockIdx.x == 0 && w == 0 && l16 == 0) __hip_atomic_store((GAS u64*)(ws + WS_EPOCH), token, RLX_AGENT);
}

DI void kv_load(u32x4& k, u32x4& v0, u32x4& v1, i32x4 srd, unsigned koff, unsigned voff, unsigned soff) {
  asm volatile("buffer_load_dwordx4 %0, %3, %5, %6 offen sc1\n\t"
               "s_mov_b64 exec, %7\n\t"
               "buffer_load_dwordx4 %1, %4, %5, %6 offen offset:1024 sc1\n\t"
               "buffer_load_dwordx4 %2, %4, %5, %6 offen offset:1536 sc1\n\t"
               "s_mov_b64 exec, -1"
               : "=&v"(k), "+&v"(v0), "+&v"(v1) : "v"(koff), "v"(voff), "s"(srd), "s"(soff), "s"(0x0000ffff0000ffffull) : "memory");
}
DI void kv_load2(u32x4& ka, u32x4& a0, u32x4& a1, u32x4& kb, u32x4& b0, u32x4& b1, i32x4 srd, unsigned koff, unsigned voff, unsigned soff) {
  asm volatile("buffer_load_dwordx4 %0, %6, %8, %9 offen sc1\n\tbuffer_load_dwordx4 %3, %6, %8, %9 offen offset:2048 sc1\n\t"
               "s_mov_b64 exec, %10\n\t"
               "buffer_load_dwordx4 %1, %7, %8, %9 offen offset:1024 sc1\n\tbuffer_load_dwordx4 %2, %7, %8, %9 offen offset:1536 sc1\n\t"
               "buffer_load_dwordx4 %4, %7, %8, %9 offen offset:3072 sc1\n\tbuffer_load_dwordx4 %5, %7, %8, %9 offen offset:3584 sc1\n\t"
               "s_mov_b64 exec, -1"
               : "=&v"(ka), "+&v"(a0), "+&v"(a1), "=&v"(kb), "+&v"(b0), "+&v"(b1) : "v"(koff), "v"(voff), "s"(srd), "s"(soff), "s"(0x0000ffff0000ffffull) : "memory");
}
DI void kv_wait2(u32x4& ka, u32x4& a0, u32x4& a1, u32x4& kb, u32x4& b0, u32x4& b1) {
  asm volatile("s_waitcnt vmcnt(0)" : "+v"(ka), "+v"(a0), "+v"(a1), "+v"(kb), "+v"(b0), "+v"(b1) :: "memory");
}
DI void v_lds4(u32x4& v0, u32x4& v1, u32x4& v2, u32x4& v3, ldsp p) {
  asm volatile("s_mov_b64 exec, %5\n\tds_read_b128 %0, %4 offset:1024\n\tds_read_b128 %1, %4 offset:1536\n\t"
               "ds_read_b128 %2, %4 offset:%6\n\tds_read_b128 %3, %4 offset:%7\n\ts_mov_b64 exec, -1"
               : "+&v"(v0), "+&v"(v1), "+&v"(v2), "+&v"(v3) : "v"(p), "s"(0x0000ffff0000ffffull), "i"(1024 + SCRW), "i"(1536 + SCRW) : "memory");
}
template <int N> DI void kv_wait(u32x4& k, u32x4& v0, u32x4& v1) { asm volatile("s_waitcnt vmcnt(%3)" : "+v"(k), "+v"(v0), "+v"(v1) : "i"(N) : "memory"); }
DI float max3(float a, float b, float c) { float r = __builtin_elementwise_maximum(__builtin_elementwise_maximum(a, b), c); asm("" : "+v"(r)); return r; }
DI float tilemax(const f32x16& s, float m) {
  float a = max3(m, s[0], s[1]), b = max3(s[2], s[3], s[4]);
  a = max3(a, s[5], s[6]); b = max3(b, s[7], s[8]); a = max3(a, s[9], s[10]); b = max3(b, s[11], s[12]); a = max3(a, s[13], s[14]);
  return max3(a, b, s[15]);
}
DI float halfmax(float m) {
  auto rr = __builtin_amdgcn_permlane32_swap(__float_as_uint(m), __float_as_uint(m), false, false);
  return __builtin_elementwise_maximum(__uint_as_float(rr[0]), __uint_as_float(rr[1]));
}

#define E_(SC, i) do { float t_ = EX2(SC[i]); PIN(t_); SC[i] = t_; SBAR(); } while (0)
#define C_(dst, j, SC, i) do { unsigned t_ = cvtpk0(SC[i], SC[i + 1]); PIN(t_); dst[j] = t_; SBAR(); } while (0)
#define UNIT(SC, SN, KN, QN, CN, OP, VAP, VBP) do { \
  OP = MFMA(VAP, pl, OP); SBAR(); \
  E_(SC, 0); C_(ph, 2, SN, 12); E_(SC, 1); C_(ph, 3, SN, 14); SN = MFMA(KN, QN, CN); SBAR(); \
  E_(SC, 2); E_(SC, 3); E_(SC, 4); \
  OP = MFMA(VBP, ph, OP); SBAR(); \
  C_(pl, 0, SC, 0); E_(SC, 5); C_(pl, 1, SC, 2); E_(SC, 6); E_(SC, 7); C_(pl, 2, SC, 4); E_(SC, 8); E_(SC, 9); C_(pl, 3, SC, 6); E_(SC, 10); E_(SC, 11); \
  C_(ph, 0, SC, 8); E_(SC, 12); E_(SC, 13); C_(ph, 1, SC, 10); E_(SC, 14); E_(SC, 15); \
} while (0)

DI void attn_waves(const Args& A, ldsp lds, const int a, const int lane, const int rb, const int cs, const int rot) {
  char* ws = A.ws;
  const unsigned lds0 = (unsigned)(uintptr_t)lds;
  u64 ep;
  const int e = a & 1, u = a >> 1, head = 2 * cs + e;
  const i32x4 srd = make_srd(ws + WS_KV + (size_t)head * 64 * 2048, 64 * 2048);
  const unsigned koff = lane * 16, voff = (lane & 15) * 16 + (lane >> 5) * 256;
  const unsigned ones = ((lane & 27) == 16) ? 0x3f803f80u : 0u;
  const u32x4 ones4 = {ones, ones, ones, ones};
  u32x4 k0, k1, k2, k3, va0, vb0, va1, vb1, va2, vb2, va3, vb3;
  f32x16 c[4], o[4];
  u32x4 bz[4], asel = {lane < 32 ? 0x3f803f80u : 0u, 0, 0, 0};
  const unsigned tbase = (unsigned)(4 * rb + 2 * u) * 2048;
#define POFF(p) ((tbase + (unsigned)(p) * 8192u) & (64u * 2048u - 1u))
  ldsp pk0, pk2, pv0, rva, rvb, pq0, pq2;
  {
    const i32x4 srdQ = make_srd(A.wqkv, DM * 3 * DM * 2), srdX = make_srd(A.x, SEQ * DM * 2);
    const unsigned xvo = off_x(rb, a, lane), vkv = off_kv(cs, a, lane), vq = off_q(cs, a, lane);
    const unsigned lbx = lds0 + a * XRING, lbk = lds0 + a * PIECE, lbq = lds0 + a * 1024; unsigned kb = 0;
    auto ldp = [&](auto m, auto i) {
      if constexpr (i == 0) kb = (m + sfresh(rot)) & 7;
      if constexpr (i < 4) dma16<L_X + (m % 3) * XBLK + i * 1152, i * 8192>(lbx, xvo, srdX, kb * 128);
      else if constexpr (i < 6) dma16<L_WB + (m % 3) * WBLK + (i - 4) * 1088, (i - 4) * 2 * 3072>(lbk, vkv, srdQ, kb * (64 * 3072));
      else dma16<L_WB + (m % 3) * WBLK + 4 * PIECE, 0>(lbq, vq, srdQ, kb * (64 * 3072)); };
    auto ld0 = [&](auto i) { SBAR(); ldp(IC<i / 7>{}, IC<i % 7>{}); SBAR(); };
    const int ln = fresh_lane(), t32 = ln & 31, hh = ln >> 5, cg = (ln >> 4) & 1, q4 = (ln >> 2) & 3, p4 = ln & 3;
    if (a == 0 && ln < 16) lds_st(lds + L_PROG + ln * 4, 0u);
    ldsp xa[4], bq[4];
    sfor<4>([&](auto i) { const int k = (i + a) & 3;
      xa[i] = opaque(lds + L_X + a * XRING + (t32 >> 3) * 1152 + (t32 & 7) * 128 + ((2 * k + hh) ^ (t32 & 7)) * 16);
      bq[i] = opaque(lds + L_WB + 4 * PIECE + k * 1024 + (8 * hh + q4) * 64 + cg * 32 + p4 * 8); });
    ld0(IC<7>{});
    ldsp bkv = opaque(lds + L_WB + a * PIECE + (q4 >> 1) * 1088 + (4 * hh + (q4 & 1)) * 128 + cg * 32 + p4 * 8);
    u32x4 sel = sumsel(ln); PINV(sel);
    ldsp pst = opaque(lds + L_STAT + a * 384 + t32 * 4), pcw = opaque(lds + L_CSKV + ((a >> 1) * 32 + t32) * 16 + (a & 1) * 8), pqw = opaque(lds + L_SCR + a * SCRW + 4096 + koff);
    ld0(IC<8>{});
    pk0 = opaque(lds + L_SCR + 2 * u * SCRW + e * 2048 + koff); pk2 = opaque(lds + L_SCR + (2 - 2 * u) * SCRW + e * 2048 + koff); pv0 = opaque(lds + L_SCR + 2 * u * SCRW + e * 2048 + voff);
    rva = opaque(lds + L_PROG + 4); rvb = opaque(lds + L_PROG + 8);
    pq2 = opaque(lds + L_SCR + 2 * u * SCRW + 4096 + e * 1024 + koff); pq0 = opaque(lds + L_SCR + (2 - 2 * u) * SCRW + 4096 + e * 1024 + koff);
    const i32x4 srdG = make_srd(A.wg, DM * FF * 2), srdU = make_srd(A.wu, DM * FF * 2);
    unsigned voffA = (unsigned)((4 * (ln >> 4) + ((ln >> 3) & 1)) * (FF * 2) + (ln & 7) * 16); PIN(voffA);
    ld0(IC<9>{});
    va0 = vb0 = va1 = vb1 = va2 = vb2 = va3 = vb3 = ones4;
    PINV(va0); PINV(vb0); PINV(va1); PINV(vb1); PINV(va2); PINV(vb2); PINV(va3); PINV(vb3); PINV(asel);
    sfor<4>([&](auto i) { ld0(IC<i + 10>{}); if constexpr (i < 3) sfor<(i == 2 ? 2 : 1)>([&](auto k) { o[i + k] = zero16(); PIN16(o[i + k]); bz[i + k] = (u32x4){0, 0, 0, 0}; PINV(bz[i + k]); }); });
    const f32x4 z4 = {0.f, 0.f, 0.f, 0.f};
    f32x4 cK = z4, cV = z4, cQ = z4, rsum = z4; f32x16 accQ = zero16();
    sfor<8>([&](auto m) {
      wait_vm<(m < 7 ? 7 : 8)>();
      wg_barrier();
      auto ld = [&](auto i) { SBAR();
        if constexpr (m + 2 < 8) ldp(IC<m + 2>{}, i);
        if constexpr (m == 6 && i < 4) { const unsigned sb = ((rot + (i >> 1)) & 7) * (64 * FF * 2) + a * (16 * FF * 2) + cs * 128;
          dma_p0<(i >> 1), 2 * (i & 1)>(lds0 + a * 4224, sb, srdG, srdU, voffA); dma_p0<(i >> 1), 2 * (i & 1) + 1>(lds0 + a * 4224, sb, srdG, srdU, voffA); }
        SBAR(); };
      u32x4 xq[4], fq[4], fk, fv;
      auto rd = [&](auto i) { xq[i] = *(LAS u32x4*)(xa[i] + (m % 3) * XBLK); fq[i] = wfrag(bq[i] + (m % 3) * WBLK, bq[i] + (m % 3) * WBLK + 256); };
      ld(IC<0>{}); rd(IC<0>{}); rd(IC<1>{});
      ld(IC<1>{}); rd(IC<2>{}); rd(IC<3>{});
      ld(IC<2>{}); { ldsp b = bkv + (m % 3) * WBLK; fk = wfrag(b, b + 256); fv = wfrag(b + 64, b + 320); }
      SBAR(); rsum = MFMA16(sel, xq[0], rsum); cQ = MFMA16(sel, fq[0], cQ); rsum = MFMA16(sel, xq[1], rsum);
      ld(IC<3>{}); rsum = MFMA16(sel, xq[2], rsum); cK = MFMA16(sel, fk, cK); rsum = MFMA16(sel, xq[3], rsum); cV = MFMA16(sel, fv, cV);
      SBAR(); accQ = MFMA(fq[0], xq[0], accQ);
      ld(IC<4>{}); accQ = MFMA(fq[1], xq[1], accQ);
      if constexpr (m == 7) { SBAR(); *(LAS float*)(pst + 128) = rsum[0] * (-1.f / DM); *(LAS u32x2*)(pcw + 2048) = split_a(cQ[0]); }
      ld(IC<5>{}); accQ = MFMA(fq[2], xq[2], accQ);
      if constexpr (m == 7) { SBAR(); *(LAS u32x2*)(pcw + hh * 1024) = split_a(hh ? cV[0] : cK[0]); }
      ld(IC<6>{}); accQ = MFMA(fq[3], xq[3], accQ);
      SBAR();
    });
    const float nmean = rsum[0] * (-1.f / DM);
    u32x4 nmf; { const u32x2 t = split_b(nmean); nmf = (u32x4){t[0], t[1], t[0], t[1]}; PINV(nmf); }
    wg_barrier();
    const u32x4 cq = *(LAS u32x4*)(lds + L_CSKV + 2048 + koff); const float ex2 = *(LAS float*)(pst + 256);
    SBAR();
    accQ = MFMA(cq, nmf, accQ);
    const float qs = __builtin_amdgcn_rsqf(fmaxf(ex2 - nmean * nmean, 0.f) + 1e-5f) * (0.25f * LOG2E);
    *(LAS u32x4*)pqw = pack8s(accQ, 0, qs);
    *(LAS u32x4*)(pqw + 1024) = pack8s(accQ, 8, qs);
    SBAR();
    ep = *(LAS u64*)(lds + L_TOK);
    wait_vm<0>();
    wg_barrier();
  }
  PRIO(1);
  u32x4 q[4];
  float chk = 0.f;
  {
    k0 = *(LAS u32x4*)pk0; q[0] = *(LAS u32x4*)pq0; SBAR(); k2 = *(LAS u32x4*)pk2; q[1] = *(LAS u32x4*)(pq0 + SCRW); SBAR();
    q[2] = *(LAS u32x4*)pq2; q[3] = *(LAS u32x4*)(pq2 + SCRW); k1 = *(LAS u32x4*)(pk0 + SCRW);
    SBAR();
    float m[4];
    auto qk = [&](auto j) { c[j & 3] = MFMA((j & 1) ? k2 : k0, q[j >> 1], zero16()); SBAR(); };
    sfor<4>([&](auto j) { qk(j); });
    sfor<8>([&](auto j) {
      m[j >> 1] = tilemax(c[j & 3], (j & 1) ? m[j >> 1] : -INFINITY); SBAR();
      if constexpr (j + 4 < 8) qk(IC<j + 4>{});
    });
    wait_word(rvb, 4 * (lds0 + L_PROG + 8)); v_lds4(va0, vb0, va1, vb1, pv0);
    sfor<4>([&](auto i) { m[i] = -102.f - halfmax(m[i]); bz[i][0] = split_a(m[i])[0]; });
    chk = max3(fabsf(m[2]), fabsf(m[3]), 0.f) * 1e31f;
    sfor<4>([&](auto i) { c[i] = MFMA(asel, bz[i], zero16()); });
    asm volatile("s_waitcnt vmcnt(0) lgkmcnt(0)" : "+v"(va0), "+v"(vb0), "+v"(va1), "+v"(vb1) :: "memory");
    rv_signal(rva);
  }
  const u64 token = ((u64)(unsigned)RFL((unsigned)(ep >> 32)) << 32) | (u64)(unsigned)RFL((unsigned)ep);
  {
    u32x4 pl = {0, 0, 0, 0}, ph = {0, 0, 0, 0};
    f32x16 sa = MFMA(k0, q[0], c[0]), sb = zero16();
    u64 flv; unsigned fo = (lane & 15) * 128, rvs;
#define STEP(KC, VAC, VBC, KN, VAP, VBP, X0, X1, X2) do { \
      UNIT(sa, sb, KC, q[1], c[1], o[3], VAP, VBP); X0; SBAR(); \
      UNIT(sb, sa, KC, q[2], c[2], o[0], VAC, VBC); X1; SBAR(); \
      UNIT(sa, sb, KC, q[3], c[3], o[1], VAC, VBC); X2; SBAR(); \
      UNIT(sb, sa, KN, q[0], c[0], o[2], VAC, VBC); \
    } while (0)
#define LD01(p) kv_load2(k0, va0, vb0, k1, va1, vb1, srd, koff, voff, POFF(p))
#define LD23(p) kv_load2(k2, va2, vb2, k3, va3, vb3, srd, koff, voff, POFF(p))
    STEP(k0, va0, vb0, k1, va1, vb1, , ldflag(flv, fo, ws + WS_FLAG + (size_t)(FLAG_KV + cs * 16) * 128), rvs = lds_ld(rva));
    wait_word(rva, 4 * (lds0 + L_PROG + 4), rvs);
    wait_vm64<0>(flv);
    wait_flags(ws, FLAG_KV + cs * 16, fo, token, flv);
    LD23(1);
    SBAR();
    for (int p = 2; p < 16; p += 2) {
      STEP(k1, va1, vb1, k2, va0, vb0, , , kv_wait2(k2, va2, vb2, k3, va3, vb3));
      STEP(k2, va2, vb2, k3, va1, vb1, LD01(p), , );
      STEP(k3, va3, vb3, k0, va2, vb2, , , kv_wait2(k0, va0, vb0, k1, va1, vb1));
      STEP(k0, va0, vb0, k1, va3, vb3, LD23(p + 1), , );
    }
    STEP(k1, va1, vb1, k2, va0, vb0, , , kv_wait2(k2, va2, vb2, k3, va3, vb3));
    STEP(k2, va2, vb2, k3, va1, vb1, , , );
    ldsp mine = opaque(lds + L_XCH + a * 6144 + koff), xf1 = opaque(lds + L_XF + a * 4);
    auto send = [&](auto i) {
      *(LAS f32x4*)(mine + i * 3072) = (f32x4){o[i][0], o[i][1], o[i][2], o[i][3]}; *(LAS f32x4*)(mine + i * 3072 + 1024) = (f32x4){o[i][4], o[i][5], o[i][6], o[i][7]};
      *(LAS float*)(mine + i * 3072 + 2048) = o[i][8]; if constexpr (i == 1) rv_signal(xf1); };
    STEP(k3, va3, vb3, k3, va2, vb2, , send(IC<0>{}), send(IC<1>{}));
    o[3] = MFMA(va3, pl, o[3]);
    ph[2] = cvtpk0(sb[12], sb[13]); ph[3] = cvtpk0(sb[14], sb[15]);
    o[3] = MFMA(vb3, ph, o[3]);
    SBAR();
  }
  {
    ldsp other = lds + L_XCH + (a ^ 2) * 6144 + koff;
    wait_word(lds + L_XF + (a ^ 2) * 4, 1u);
    f32x4 x0[2], x1[2]; float l2[2], big[2];
    sfor<2>([&](auto i) { l2[i] = *(LAS float*)(other + i * 3072 + 2048); x0[i] = *(LAS f32x4*)(other + i * 3072); x1[i] = *(LAS f32x4*)(other + i * 3072 + 1024); });
    SBAR();
    sfor<2>([&](auto i) {
      sfor<9>([&](auto r) { float t = o[2 + i][+r] + (r < 4 ? x0[i][r & 3] : r < 8 ? x1[i][r & 3] : l2[i]); PINF(t); o[2 + i][+r] = t; });
      float t = max3(fabsf(o[2 + i][0]), fabsf(o[2 + i][1]), fabsf(o[2 + i][2])), t2 = max3(fabsf(o[2 + i][3]), fabsf(o[2 + i][4]), fabsf(o[2 + i][5]));
      t = max3(t, fabsf(o[2 + i][6]), fabsf(o[2 + i][7])); big[i] = max3(t, t2, t2); });
    chk = max3(max3(chk, o[2][8], o[3][8]), big[0], big[1]);
    if (__builtin_expect(!__all(chk < 1e36f && fminf(big[0], big[1]) > 2e-34f), 0)) {
      float m2 = -INFINITY, m3 = -INFINITY;
      for (int i = 0; i < 64; ++i) {
        kv_load(k0, va0, vb0, srd, koff, voff, (unsigned)i * 2048u); kv_wait<0>(k0, va0, vb0);
        m2 = tilemax(MFMA(k0, q[2], zero16()), m2); m3 = tilemax(MFMA(k0, q[3], zero16()), m3);
      }
      m2 = -halfmax(m2); m3 = -halfmax(m3);
      for (int r = 0; r < 16; ++r) { c[2][r] = m2; c[3][r] = m3; o[2][r] = 0.f; o[3][r] = 0.f; }
      for (int i = 0; i < 64; ++i) {
        kv_load(k0, va0, vb0, srd, koff, voff, (unsigned)i * 2048u); kv_wait<0>(k0, va0, vb0);
        sfor<2>([&](auto t) { f32x16 sc = MFMA(k0, q[2 + t], c[2 + t]); for (int r = 0; r < 16; ++r) sc[r] = EX2(sc[r]);
          o[2 + t] = MFMA(va0, pack8(sc, 0), o[2 + t]); o[2 + t] = MFMA(vb0, pack8(sc, 8), o[2 + t]); });
      }
    }
    const i32x4 ro = make_srd(ws + WS_O, 64 * 32 * 1024);
    sfor<2>([&](auto i) { st_sc1(pack8s(o[2 + i], 0, __builtin_amdgcn_rcpf(o[2 + i][8])), ro, koff, (unsigned)(((4 * rb + 2 * u + i) * 32 + head) * 1024)); });
  }
  wait_vm<0>();
  arrive_and_flag(lds + L_CNT + 8, ws, FLAG_O + rb * 16 + cs, token);
}

__global__ __launch_bounds__(512, 2) void fused_block_kernel(Args A) {
  extern __shared__ __attribute__((aligned(16))) char smem[];
  ldsp lds = (ldsp)smem;
  const int lane = threadIdx.x & 63, wid = RFL(threadIdx.x >> 6);
  const int b = blockIdx.x, g = b & 7, j = b >> 3;
  const int rb = 8 * (g >> 2) + (j & 7), cs = 4 * (g & 3) + (j >> 3), tb = 4 * g + (j & 3), nb = j >> 2;
  const int rot = (rb + 2 * (cs >> 2) + 4 * (cs & 1)) & 7;
  u64 pc; asm volatile("s_getpc_b64 %0" : "=s"(pc));
  if (wid < 4) {
    const unsigned xvo = off_x(rb, wid, lane), vkv = off_kv(cs, wid, lane), vq = off_q(cs, wid, lane);
    const unsigned lds0 = (unsigned)(uintptr_t)lds; const i32x4 srdQ = make_srd(A.wqkv, DM * 3 * DM * 2), srdX = make_srd(A.x, SEQ * DM * 2);
    sfor<4>([&](auto i) { dma16<L_X + i * 1152, i * 8192>(lds0 + wid * XRING, xvo, srdX, rot * 128); });
    sfor<2>([&](auto i) { dma16<L_WB + i * 1088, i * 2 * 3072>(lds0 + wid * PIECE, vkv, srdQ, rot * (64 * 3072)); });
    dma16<L_WB + 4 * PIECE, 0>(lds0 + wid * 1024, vq, srdQ, rot * (64 * 3072));
  }
  const int wid2 = sfresh(wid);
  if (wid2 < 4) { PRIO(1); attn_waves(A, lds, wid2, fresh_lane(), rb, cs, rot); __builtin_amdgcn_endpgm(); }
  PRIO(2); gemm_waves(A, lds, wid2 - 4, fresh_lane(), rb, cs, tb, nb, rot, pc);
}

extern "C" void kernel_launch(void* const* d_in, const int* in_sizes, int n_in, void* d_out, int out_size, void* d_ws, size_t ws_size, hipStream_t stream) {
  Args A;
  A.x = (const char*)d_in[0]; A.wqkv = (const char*)d_in[1]; A.wo = (const char*)d_in[2];
  A.wg = (const char*)d_in[4]; A.wu = (const char*)d_in[5]; A.wd = (const char*)d_in[6];
  A.out = (char*)d_out; A.ws = (char*)d_ws;
  (void)hipFuncSetAttribute((const void*)fused_block_kernel, hipFuncAttributeMaxDynamicSharedMemorySize, LDS_BYTES);
  fused_block_kernel<<<256, 512, LDS_BYTES, stream>>>(A);
}
```
